```python
import torch

def _softmax(x, axis=-1):
    return torch.softmax(x, dim=axis)
def _gelu(x):
    return torch.nn.functional.gelu(x, approximate="tanh")
def _silu(x):
    return torch.nn.functional.silu(x)
def _sigmoid(x):
    return torch.sigmoid(x)
def _layernorm(x, axis=-1, eps=1e-5):
    m = x.mean(dim=axis, keepdim=True)
    v = x.var(dim=axis, keepdim=True, unbiased=False)
    return (x - m) / torch.sqrt(v + eps)
def _scatter_add(tgt, idx, src, axis=0):
    return tgt.clone().index_add_(axis, idx, src)
def _segment_sum(src, seg, n):
    out = torch.zeros((n,) + src.shape[1:], dtype=src.dtype)
    return out.index_add_(0, seg, src)
def _pad(x, pw):
    flat = tuple(v for pair in reversed(pw) for v in pair)
    return torch.nn.functional.pad(x, flat)

kernel_name = 'concat_heads__mha_block_cached__mha_block_cached__near_ridge'

def setup_inputs():

    _w = (lambda a: (lambda t: t.to(torch.bfloat16) if t.is_floating_point() else t)(torch.as_tensor(a)))
    def _A_setup():
        B, Sq, D = 8, 192, 512
        H, Sk, Dh = 32, 1024, 128
        return {
            "x": _w((lambda *s: torch.randn(*s, dtype=torch.bfloat16) * 0.8)(B, Sq, D)),
            "k_cache": _w((lambda *s: torch.randn(*s, dtype=torch.bfloat16) * 0.8)(B, H, Sk, Dh)),
            "v_cache": _w((lambda *s: torch.randn(*s, dtype=torch.bfloat16) * 0.05)(B, H, Sk, Dh)),
            "wq": _w((lambda *s: torch.randn(*s, dtype=torch.bfloat16) * 0.8)(D, H * Dh) * (D ** -0.5)),
            "wo": _w((lambda *s: torch.randn(*s, dtype=torch.bfloat16) * 0.05)(H * Dh, D) * ((H * Dh) ** -0.5)),
        }
    _A_in = _A_setup()

    def _B_setup():
        B, Sq, D = 8, 192, 512
        H, Sk, Dh = 32, 1024, 128
        return {
            "x": _w((lambda *s: torch.randn(*s, dtype=torch.bfloat16) * 0.8)(B, Sq, D)),
            "k_cache": _w((lambda *s: torch.randn(*s, dtype=torch.bfloat16) * 0.8)(B, H, Sk, Dh)),
            "v_cache": _w((lambda *s: torch.randn(*s, dtype=torch.bfloat16) * 0.05)(B, H, Sk, Dh)),
            "wq": _w((lambda *s: torch.randn(*s, dtype=torch.bfloat16) * 0.8)(D, H * Dh) * (D ** -0.5)),
            "wo": _w((lambda *s: torch.randn(*s, dtype=torch.bfloat16) * 0.05)(H * Dh, D) * ((H * Dh) ** -0.5)),
        }
    _B_in = _B_setup()

    return {
        "A_x": _A_in["x"],
        "A_k_cache": _A_in["k_cache"],
        "A_v_cache": _A_in["v_cache"],
        "A_wq": _A_in["wq"],
        "A_wo": _A_in["wo"],
        "B_x": _B_in["x"],
        "B_k_cache": _B_in["k_cache"],
        "B_v_cache": _B_in["v_cache"],
        "B_wq": _B_in["wq"],
        "B_wo": _B_in["wo"],
    }

def reference(A_x, A_k_cache, A_v_cache, A_wq, A_wo, B_x, B_k_cache, B_v_cache, B_wq, B_wo):
    def _A_ref(x, k_cache, v_cache, wq, wo):
        B, Sq, D = x.shape
        H, Dh = 32, 128
        q = (lambda x, *d: x.permute(*d))((x @ wq).reshape(B, Sq, H, Dh), 0, 2, 1, 3)
        scores = torch.einsum("bhqd,bhkd->bhqk", q, k_cache) * (Dh ** -0.5)
        p = _softmax(scores, axis=-1)
        o = torch.einsum("bhqk,bhkd->bhqd", p, v_cache)
        return (lambda x, *d: x.permute(*d))(o, 0, 2, 1, 3).reshape(B, Sq, H * Dh) @ wo

    def _B_ref(x, k_cache, v_cache, wq, wo):
        B, Sq, D = x.shape
        H, Dh = 32, 128
        q = (lambda x, *d: x.permute(*d))((x @ wq).reshape(B, Sq, H, Dh), 0, 2, 1, 3)
        scores = torch.einsum("bhqd,bhkd->bhqk", q, k_cache) * (Dh ** -0.5)
        p = _softmax(scores, axis=-1)
        o = torch.einsum("bhqk,bhkd->bhqd", p, v_cache)
        return (lambda x, *d: x.permute(*d))(o, 0, 2, 1, 3).reshape(B, Sq, H * Dh) @ wo

    a = _A_ref(A_x, A_k_cache, A_v_cache, A_wq, A_wo)
    b = _B_ref(B_x, B_k_cache, B_v_cache, B_wq, B_wo)
    return torch.concatenate([a, b], axis=-1)
```

```cpp
#include <hip/hip_runtime.h>
#include <hip/hip_bf16.h>
typedef __hip_bfloat16 bf16;
__device__ inline float b2f(unsigned short u){ return __uint_as_float(((unsigned)u)<<16); }
__device__ inline unsigned short f2b(float f){ __hip_bfloat16 h = __float2bfloat16(f); return *(unsigned short*)&h; }

__global__ void k_qproj(const unsigned short* x, const unsigned short* wq, float* q) {
  int col = blockIdx.x*256 + threadIdx.x, row = blockIdx.y;
  float acc = 0.f;
  for (int c = 0; c < 512; ++c) acc += b2f(x[row*512+c]) * b2f(wq[(size_t)c*4096+col]);
  q[(size_t)row*4096+col] = b2f(f2b(acc));
}
__global__ void k_attn(const float* q, const unsigned short* K, const unsigned short* V, float* o) {
  __shared__ float sq[128]; __shared__ float sp[1024]; __shared__ float red[256];
  int s = blockIdx.x, h = blockIdx.y, b = blockIdx.z, t = threadIdx.x;
  const float* qr = q + ((size_t)(b*192+s))*4096 + h*128;
  if (t < 128) sq[t] = qr[t];
  __syncthreads();
  const unsigned short* Kh = K + ((size_t)(b*32+h))*1024*128;
  const unsigned short* Vh = V + ((size_t)(b*32+h))*1024*128;
  float mx = -1e30f;
  for (int i = 0; i < 4; ++i) { int k = t + 256*i; float a = 0.f;
    for (int d = 0; d < 128; ++d) a += sq[d]*b2f(Kh[k*128+d]);
    a *= 0.08838834764831845f; sp[k] = a; mx = fmaxf(mx, a); }
  red[t] = mx; __syncthreads();
  for (int o2 = 128; o2 > 0; o2 >>= 1) { if (t < o2) red[t] = fmaxf(red[t], red[t+o2]); __syncthreads(); }
  mx = red[0]; __syncthreads();
  float sm = 0.f;
  for (int i = 0; i < 4; ++i) { int k = t + 256*i; float e = __expf(sp[k]-mx); sp[k] = e; sm += e; }
  red[t] = sm; __syncthreads();
  for (int o2 = 128; o2 > 0; o2 >>= 1) { if (t < o2) red[t] += red[t+o2]; __syncthreads(); }
  sm = red[0];
  if (t < 128) { float a = 0.f; for (int k = 0; k < 1024; ++k) a += sp[k]*b2f(Vh[k*128+t]);
    o[((size_t)(b*192+s))*4096 + h*128 + t] = b2f(f2b(a/sm)); }
}
__global__ void k_oproj(const float* o, const unsigned short* wo, unsigned short* out, int coff) {
  int col = blockIdx.x*256 + threadIdx.x, row = blockIdx.y;
  float acc = 0.f;
  for (int c = 0; c < 4096; ++c) acc += o[(size_t)row*4096+c] * b2f(wo[(size_t)c*512+col]);
  out[(size_t)row*1024 + coff + col] = f2b(acc);
}
extern "C" void kernel_launch(void* const* d_in, const int* in_sizes, int n_in, void* d_out, int out_size, void* d_ws, size_t ws_size, hipStream_t stream) {
  float* q = (float*)d_ws; float* o = q + (size_t)1536*4096;
  for (int blk = 0; blk < 2; ++blk) {
    const unsigned short* x = (const unsigned short*)d_in[blk*5+0];
    const unsigned short* K = (const unsigned short*)d_in[blk*5+1];
    const unsigned short* V = (const unsigned short*)d_in[blk*5+2];
    const unsigned short* wq = (const unsigned short*)d_in[blk*5+3];
    const unsigned short* wo = (const unsigned short*)d_in[blk*5+4];
    k_qproj<<<dim3(16,1536),256,0,stream>>>(x,wq,q);
    k_attn<<<dim3(192,32,8),256,0,stream>>>(q,K,V,o);
    k_oproj<<<dim3(2,1536),256,0,stream>>>(o,wo,(unsigned short*)d_out,blk*512);
  }
}
```

```cpp
#include <hip/hip_runtime.h>
#include <hip/hip_bf16.h>
#define O_OFF (512u << 10)
#define WS_BASE(n) ((size_t)(200u << 20))
#define PROF_OFF(n) (WS_BASE(n) + (52u << 20))

__global__ __launch_bounds__(512, 2) void mha_kernel(const char* xA, const char* kA, const char* vA, const char* wqA, const char* woA,
                                                     const char* xB, const char* kB, const char* vB, const char* wqB, const char* woB,
                                                     char* ws, char* out) {
  __shared__ __attribute__((aligned(128))) char lds[159488];
  const unsigned wg = blockIdx.x, xcd = wg & 7u, idx = wg >> 3, b = idx & 7u, pr = xcd * 4u + (idx >> 3), blk = pr >> 4, j = pr & 15u, g16 = blk * 8u + b;
  const unsigned wave = __builtin_amdgcn_readfirstlane(threadIdx.x >> 6), team = wave >> 2, w = wave & 3u, h = 2u * j + team;
  const char* kp = (blk ? kB : kA) + (size_t)(b * 32u + h) * 262144u;
  const char* vp = (blk ? vB : vA) + (size_t)(b * 32u + h) * 262144u;
  const unsigned kr = ((wg * 2u + team) * 2654435761u) >> 27, kst = (((kr >> 0) & 1u) << 10) | (((kr >> 1) & 1u) << 11) | (((kr >> 2) & 1u) << 12) | (((kr >> 3) & 1u) << 14) | (((kr >> 4) & 1u) << 17);
  const char* wp = (blk ? wqB : wqA) + h * 256u;
  const char* xp = (blk ? xB : xA) + (size_t)(b * 192u + w * 48u) * 1024u;
  const unsigned op = O_OFF + ((g16 * 32u + h) * 12u + 3u * w) * 4096u;
  const unsigned wsz = 512u * 8192u - h * 256u;
  const unsigned ldsb = (unsigned)(size_t)lds;
  const unsigned cq = idx >> 3, kq = xcd & 3u;
  const unsigned op2 = O_OFF + ((g16 * 32u + 8u * kq) * 12u + 3u * w) * 4096u;
  const char* wp2 = (blk ? woB : woA) + (size_t)kq * 1048576u + cq * 256u + w * 2048u;
  char* rp = out + ((size_t)(b * 192u + 48u * w) * 1024u + blk * 512u + cq * 128u + kq * 32u + team * 16u) * 2u;
  const unsigned htab = (((0x33210u + cq * 0x11111u) & 0x33333u) << 1) | (team ? 0x11111u : 0x10000u);
  const unsigned q = j & 3u;
  const unsigned dta = team ? (unsigned)((6367353648857972875ull >> (8u * b)) & 0xffu) + 5505088u
                            : (unsigned)((7522528018143336790ull >> (8u * b)) & 0xffu) + 11141120u;
  const unsigned nsw = 16u + (team ? (unsigned)((1301839424133073931ull >> (8u * b)) & 0xffu) : 28u);
  const unsigned kan = team ? (unsigned)((1013046106685445399ull >> (8u * b)) & 0xffu) : 9u;
  asm volatile(
    "s_mov_b64 s[28:29], %[wp]\n"
    "s_and_b32 s29, s29, 0xffff\n"
    "s_mov_b32 s30, %[wsz]\n"
    "s_mov_b32 s31, 0x20000\n"
    "s_mov_b64 s[68:69], %[xp]\n"
    "s_and_b32 s69, s69, 0xffff\n"
    "s_mov_b32 s70, 0xc000\n"
    "s_mov_b32 s71, 0x20000\n"
    "v_mbcnt_lo_u32_b32 v27, -1, 0\n"
    "v_mbcnt_hi_u32_b32 v27, -1, v27\n"
    "s_mul_i32 s54, %[team], 76032\n"
    "s_add_u32 s54, s54, %[lds]\n"
    "s_mul_i32 s55, %[w], 2112\n"
    "s_add_u32 s40, s54, s55\n"
    "s_lshr_b32 s55, %[w], 1\n"
    "s_lshl_b32 s55, s55, 3\n"
    "s_and_b32 s56, %[w], 1\n"
    "s_lshl_b32 s56, s56, 1\n"
    "s_add_u32 s55, s55, s56\n"
    "s_lshl_b32 s43, s55, 13\n"
    "v_lshrrev_b32 v28, 4, v27\n"
    "v_and_b32 v24, 15, v27\n"
    "v_lshlrev_b32 v12, 4, v24\n"
    "v_and_b32 v24, 1, v28\n"
    "v_lshlrev_b32 v24, 17, v24\n"
    "v_lshrrev_b32 v26, 1, v28\n"
    "v_lshlrev_b32 v26, 15, v26\n"
    "v_add3_u32 v12, v12, v24, v26\n"
    "s_add_u32 m0, s40, 8448\n"
    "s_add_u32 s44, s43, 8192\n"
    "buffer_load_dwordx4 v12, s[28:31], s43 offen lds\n"
    "s_add_u32 m0, s40, 9504\n"
    "s_add_u32 s43, s43, 262144\n"
    "buffer_load_dwordx4 v12, s[28:31], s44 offen lds\n"
    "v_and_b32 v24, 7, v27\n"
    "v_xor_b32 v24, v24, v28\n"
    "v_lshlrev_b32 v24, 4, v24\n"
    "v_lshrrev_b32 v25, 3, v27\n"
    "v_lshlrev_b32 v25, 10, v25\n"
    "v_add_u32 v13, v24, v25\n"
    "s_lshl_b32 s55, %[team], 6\n"
    "v_xor_b32 v13, s55, v13\n"
    "v_xor_b32 v14, 64, v13\n"
    "s_mul_i32 s48, %[w], 6144\n"
    "s_mul_i32 s55, %[team], 3072\n"
    "s_add_u32 s48, s48, s55\n"
    "s_add_u32 s48, s48, %[lds]\n"
    "s_add_u32 s48, s48, 50688\n"
    "s_mul_i32 s78, %[team], 24576\n"
    "s_add_u32 m0, s48, 0\n"
    "s_add_u32 s76, s78, 0\n"
    "buffer_load_dwordx4 v13, s[68:71], s76 offen lds\n"
    "s_add_u32 m0, s48, 1024\n"
    "s_add_u32 s77, s78, 8192\n"
    "buffer_load_dwordx4 v14, s[68:71], s77 offen lds\n"
    "s_add_u32 m0, s48, 2048\n"
    "s_add_u32 s76, s78, 16384\n"
    "buffer_load_dwordx4 v13, s[68:71], s76 offen lds\n"
    "s_add_u32 m0, s40, 16896\n"
    "s_add_u32 s44, s43, 8192\n"
    "buffer_load_dwordx4 v12, s[28:31], s43 offen lds\n"
    "s_add_u32 m0, s40, 17952\n"
    "s_add_u32 s43, s43, 262144\n"
    "buffer_load_dwordx4 v12, s[28:31], s44 offen lds\n"
    "s_add_u32 m0, s48, 76032\n"
    "s_add_u32 s76, s78, 128\n"
    "buffer_load_dwordx4 v13, s[68:71], s76 offen lds\n"
    "s_add_u32 m0, s48, 77056\n"
    "s_add_u32 s77, s78, 8320\n"
    "buffer_load_dwordx4 v14, s[68:71], s77 offen lds\n"
    "s_add_u32 m0, s48, 78080\n"
    "s_add_u32 s76, s78, 16512\n"
    "buffer_load_dwordx4 v13, s[68:71], s76 offen lds\n"
    "s_add_u32 m0, s40, 25344\n"
    "s_add_u32 s44, s43, 8192\n"
    "buffer_load_dwordx4 v12, s[28:31], s43 offen lds\n"
    "s_add_u32 m0, s40, 26400\n"
    "s_add_u32 s43, s43, 262144\n"
    "buffer_load_dwordx4 v12, s[28:31], s44 offen lds\n"
    "s_mov_b64 s[20:21], %[kp]\n"
    "s_and_b32 s21, s21, 0xffff\n"
    "s_mov_b32 s22, 0x40000\n"
    "s_mov_b32 s23, 0x20000\n"
    "s_mov_b64 s[24:25], %[vp]\n"
    "s_and_b32 s25, s25, 0xffff\n"
    "s_mov_b32 s26, 0x40000\n"
    "s_mov_b32 s27, 0x20000\n"
    "s_mov_b64 s[36:37], %[ws]\n"
    "s_add_u32 s36, s36, %[op]\n"
    "s_addc_u32 s37, s37, 0\n"
    "s_and_b32 s37, s37, 0xffff\n"
    "s_mov_b32 s38, 0x3000\n"
    "s_mov_b32 s39, 0x20000\n"
    "s_mov_b32 s59, %[kst]\n"
    "s_mov_b32 s41, s59\n"
    "s_mov_b32 s42, s59\n"
    "s_mov_b32 s60, 0\n"
    "s_mov_b32 s61, 4096\n"
    "s_mov_b32 s62, 8192\n"
    "s_mov_b32 s46, 0\n"
    "s_mov_b32 s58, 0x4b800000\n"
    "s_mov_b32 s47, 0x3e0293ee\n"
    "s_mov_b32 s49, 0\n"
    "s_mov_b32 s51, 0\n"
    "s_and_b32 s83, %[dta], 0xffff\n"
    "s_lshr_b32 s79, %[dta], 16\n"
    "s_memrealtime s[80:81]\n"
    "v_lshlrev_b32 v3, 4, v27\n"
    "v_lshrrev_b32 v11, 8, v3\n"
    "v_and_b32 v3, 0xf0, v3\n"
    "v_lshl_or_b32 v3, v11, 15, v3\n"
    "v_xor_b32 v11, %[w], v11\n"
    "v_lshl_or_b32 v3, v11, 8, v3\n"
    "s_lshl_b32 s55, %[team], 10\n"
    "s_add_u32 s55, s55, %[lds]\n"
    "s_add_u32 s55, s55, 152064\n"
    "s_lshl_b32 s56, %[w], 8\n"
    "s_add_u32 s56, s56, s55\n"
    "v_lshlrev_b32 v4, 2, v27\n"
    "v_add_u32 v4, s56, v4\n"
    "v_and_b32 v24, 3, v27\n"
    "v_mul_u32_u24 v5, 260, v24\n"
    "v_add_u32 v34, s55, v5\n"
    "v_bfe_u32 v24, v27, 2, 1\n"
    "v_lshlrev_b32 v24, 10, v24\n"
    "s_add_u32 s55, %[lds], 152064\n"
    "v_add3_u32 v5, v5, v24, s55\n"
    "v_mov_b32 v6, 0\n"
    "ds_write_b32 v4, v6\n"
    "s_mov_b32 s57, s54\n"
    "s_xor_b32 s54, %[team], 0\n"
    "s_lshl_b32 s54, s54, 2\n"
    "s_add_u32 s54, s54, %[w]\n"
    "s_lshl_b32 s54, s54, 2\n"
    "s_add_u32 s54, s54, %[lds]\n"
    "s_add_u32 s54, s54, 158208\n"
    "v_mov_b32 v24, s54\n"
    "s_mov_b32 s54, s57\n"
    "ds_write_b32 v24, v6\n"
    "v_and_b32 v24, 7, v27\n"
    "v_mul_u32_u24 v1, 1056, v24\n"
    "v_bfe_u32 v24, v27, 3, 1\n"
    "v_lshlrev_b32 v24, 8, v24\n"
    "v_lshlrev_b32 v26, 4, v28\n"
    "v_add3_u32 v1, v1, v24, v26\n"
    "v_add_u32 v1, s54, v1\n"
    "v_bfe_u32 v24, v27, 2, 3\n"
    "v_mul_u32_u24 v2, 1056, v24\n"
    "v_lshrrev_b32 v24, 5, v27\n"
    "v_lshlrev_b32 v24, 8, v24\n"
    "v_and_b32 v26, 3, v27\n"
    "v_lshlrev_b32 v26, 3, v26\n"
    "v_add3_u32 v2, v2, v24, v26\n"
    "v_add_u32 v2, s54, v2\n"
    "v_and_b32 v24, 15, v27\n"
    "v_lshrrev_b32 v25, 1, v24\n"
    "v_xor_b32 v25, v25, v28\n"
    "v_lshlrev_b32 v25, 4, v25\n"
    "v_lshlrev_b32 v24, 7, v24\n"
    "v_add_u32 v24, v24, v25\n"
    "s_mul_i32 s55, %[w], 6144\n"
    "s_add_u32 s55, s55, %[lds]\n"
    "s_add_u32 s55, s55, 50688\n"
    "v_add_u32 v30, s55, v24\n"
    "v_xor_b32 v31, 64, v30\n"
    "v_add_u32 v32, 76032, v30\n"
    "v_xor_b32 v33, 64, v32\n"
    "v_mov_b32 v6, 1\n"
    "s_waitcnt vmcnt(7) lgkmcnt(0)\n"
    "s_barrier\n"
    "s_mul_i32 s82, %[team], 40\n"
    "s_add_u32 s82, s82, 1265\n"
    "s_add_u32 s82, s82, s80\n"
    "ds_read_b128 v[144:147], v30 offset:0\n"
    "ds_read_b128 v[148:151], v30 offset:2048\n"
    "ds_read_b128 v[152:155], v30 offset:4096\n"
    "ds_read_b128 v[156:159], v31 offset:0\n"
    "ds_read_b128 v[160:163], v31 offset:2048\n"
    "ds_read_b128 v[164:167], v31 offset:4096\n"
    "ds_read_b64_tr_b16 a[0:1], v2 offset:8448\n"
    "ds_read_b64_tr_b16 a[2:3], v2 offset:8960\n"
    "ds_read_b64_tr_b16 a[4:5], v2 offset:8480\n"
    "ds_read_b64_tr_b16 a[6:7], v2 offset:8992\n"
    "ds_read_b64_tr_b16 a[8:9], v2 offset:8512\n"
    "ds_read_b64_tr_b16 a[10:11], v2 offset:9024\n"
    "ds_read_b64_tr_b16 a[12:13], v2 offset:8544\n"
    "ds_read_b64_tr_b16 a[14:15], v2 offset:9056\n"
    "ds_read_b64_tr_b16 a[16:17], v2 offset:8576\n"
    "s_waitcnt lgkmcnt(14)\n"
    "ds_read_b64_tr_b16 a[18:19], v2 offset:9088\n"
    "s_waitcnt lgkmcnt(14)\n"
    "ds_read_b64_tr_b16 a[20:21], v2 offset:8608\n"
    "s_waitcnt lgkmcnt(14)\n"
    "ds_read_b64_tr_b16 a[22:23], v2 offset:9120\n"
    "s_waitcnt lgkmcnt(14)\n"
    "ds_read_b64_tr_b16 a[24:25], v2 offset:8640\n"
    "s_waitcnt lgkmcnt(14)\n"
    "ds_read_b64_tr_b16 a[26:27], v2 offset:9152\n"
    "s_waitcnt lgkmcnt(14)\n"
    "ds_read_b64_tr_b16 a[28:29], v2 offset:8672\n"
    "s_waitcnt lgkmcnt(14)\n"
    "ds_read_b64_tr_b16 a[30:31], v2 offset:9184\n"
    "s_waitcnt vmcnt(5) lgkmcnt(0)\n"
    "s_barrier\n"
    "v_mfma_f32_16x16x32_bf16 v[48:51], a[0:3], v[144:147], 0\n"
    "s_add_u32 m0, s40, 33792\n"
    "s_add_u32 s44, s43, 8192\n"
    "buffer_load_dwordx4 v12, s[28:31], s43 offen lds\n"
    "ds_read_b64_tr_b16 a[32:33], v2 offset:16896\n"
    "ds_read_b64_tr_b16 a[34:35], v2 offset:17408\n"
    "v_mfma_f32_16x16x32_bf16 v[52:55], a[4:7], v[144:147], 0\n"
    "ds_read_b64_tr_b16 a[36:37], v2 offset:16928\n"
    "v_mfma_f32_16x16x32_bf16 v[56:59], a[8:11], v[144:147], 0\n"
    "s_add_u32 m0, s40, 34848\n"
    "s_add_u32 s43, s43, 262144\n"
    "buffer_load_dwordx4 v12, s[28:31], s44 offen lds\n"
    "ds_read_b64_tr_b16 a[38:39], v2 offset:17440\n"
    "v_mfma_f32_16x16x32_bf16 v[60:63], a[12:15], v[144:147], 0\n"
    "ds_read_b64_tr_b16 a[40:41], v2 offset:16960\n"
    "v_mfma_f32_16x16x32_bf16 v[64:67], a[16:19], v[144:147], 0\n"
    "s_add_u32 m0, s48, 0\n"
    "s_add_u32 s76, s78, 256\n"
    "buffer_load_dwordx4 v13, s[68:71], s76 offen lds\n"
    "ds_read_b64_tr_b16 a[42:43], v2 offset:17472\n"
    "v_mfma_f32_16x16x32_bf16 v[68:71], a[20:23], v[144:147], 0\n"
    "ds_read_b64_tr_b16 a[44:45], v2 offset:16992\n"
    "v_mfma_f32_16x16x32_bf16 v[72:75], a[24:27], v[144:147], 0\n"
    "s_add_u32 m0, s48, 1024\n"
    "s_add_u32 s77, s78, 8448\n"
    "buffer_load_dwordx4 v14, s[68:71], s77 offen lds\n"
    "ds_read_b64_tr_b16 a[46:47], v2 offset:17504\n"
    "v_mfma_f32_16x16x32_bf16 v[76:79], a[28:31], v[144:147], 0\n"
    "ds_read_b64_tr_b16 a[48:49], v2 offset:17024\n"
    "ds_read_b64_tr_b16 a[50:51], v2 offset:17536\n"
    "v_mfma_f32_16x16x32_bf16 v[80:83], a[0:3], v[148:151], 0\n"
    "s_add_u32 m0, s48, 2048\n"
    "s_add_u32 s76, s78, 16640\n"
    "buffer_load_dwordx4 v13, s[68:71], s76 offen lds\n"
    "ds_read_b64_tr_b16 a[52:53], v2 offset:17056\n"
    "s_waitcnt vmcnt(5)\n"
    "ds_add_u32 v4, v6\n"
    "s_add_u32 s46, s46, 1\n"
    "s_setprio 0\n"
    "v_mfma_f32_16x16x32_bf16 v[84:87], a[4:7], v[148:151], 0\n"
    "ds_read_b64_tr_b16 a[54:55], v2 offset:17568\n"
    "v_mfma_f32_16x16x32_bf16 v[88:91], a[8:11], v[148:151], 0\n"
    "s_add_u32 m0, s40, 0\n"
    "s_add_u32 s44, s43, 8192\n"
    "buffer_load_dwordx4 v12, s[28:31], s43 offen lds\n"
    "ds_read_b64_tr_b16 a[56:57], v2 offset:17088\n"
    "v_mfma_f32_16x16x32_bf16 v[92:95], a[12:15], v[148:151], 0\n"
    "ds_read_b64_tr_b16 a[58:59], v2 offset:17600\n"
    "v_mfma_f32_16x16x32_bf16 v[96:99], a[16:19], v[148:151], 0\n"
    "s_add_u32 m0, s40, 1056\n"
    "s_add_u32 s43, s43, 262144\n"
    "buffer_load_dwordx4 v12, s[28:31], s44 offen lds\n"
    "s_waitcnt lgkmcnt(14)\n"
    "ds_read_b64_tr_b16 a[60:61], v2 offset:17120\n"
    "v_mfma_f32_16x16x32_bf16 v[100:103], a[20:23], v[148:151], 0\n"
    "s_waitcnt lgkmcnt(14)\n"
    "ds_read_b64_tr_b16 a[62:63], v2 offset:17632\n"
    "v_mfma_f32_16x16x32_bf16 v[104:107], a[24:27], v[148:151], 0\n"
    "s_add_u32 m0, s40, 42240\n"
    "s_add_u32 s44, s43, 8192\n"
    "buffer_load_dwordx4 v12, s[28:31], s43 offen lds\n"
    "s_waitcnt lgkmcnt(14)\n"
    "ds_read_b32 v7, v5\n"
    "v_mfma_f32_16x16x32_bf16 v[108:111], a[28:31], v[148:151], 0\n"
    "v_mfma_f32_16x16x32_bf16 v[112:115], a[0:3], v[152:155], 0\n"
    "s_add_u32 m0, s40, 43296\n"
    "s_add_u32 s43, s43, 262144\n"
    "buffer_load_dwordx4 v12, s[28:31], s44 offen lds\n"
    "v_mfma_f32_16x16x32_bf16 v[116:119], a[4:7], v[152:155], 0\n"
    "s_waitcnt lgkmcnt(0)\n"
    "v_cmp_gt_u32 vcc, s46, v7\n"
    "v_mfma_f32_16x16x32_bf16 v[120:123], a[8:11], v[152:155], 0\n"
    "s_cbranch_vccnz Lfs_2_%=\n"
    "Lfb_1_%=:\n"
    "v_mfma_f32_16x16x32_bf16 v[124:127], a[12:15], v[152:155], 0\n"
    "s_add_u32 m0, s40, 8448\n"
    "s_add_u32 s44, s43, 8192\n"
    "buffer_load_dwordx4 v12, s[28:31], s43 offen lds\n"
    "v_mfma_f32_16x16x32_bf16 v[128:131], a[16:19], v[152:155], 0\n"
    "v_mfma_f32_16x16x32_bf16 v[132:135], a[20:23], v[152:155], 0\n"
    "v_mfma_f32_16x16x32_bf16 v[136:139], a[24:27], v[152:155], 0\n"
    "s_add_u32 m0, s40, 9504\n"
    "s_add_u32 s43, s43, 262144\n"
    "buffer_load_dwordx4 v12, s[28:31], s44 offen lds\n"
    "v_mfma_f32_16x16x32_bf16 v[140:143], a[28:31], v[152:155], 0\n"
    "s_setprio 3\n"
    "v_mfma_f32_16x16x32_bf16 v[48:51], a[32:35], v[156:159], v[48:51]\n"
    "ds_read_b128 v[168:171], v32 offset:0\n"
    "ds_read_b128 v[172:175], v32 offset:2048\n"
    "v_mfma_f32_16x16x32_bf16 v[52:55], a[36:39], v[156:159], v[52:55]\n"
    "ds_read_b128 v[176:179], v32 offset:4096\n"
    "ds_read_b128 v[180:183], v33 offset:0\n"
    "v_mfma_f32_16x16x32_bf16 v[56:59], a[40:43], v[156:159], v[56:59]\n"
    "ds_read_b128 v[184:187], v33 offset:2048\n"
    "v_mfma_f32_16x16x32_bf16 v[60:63], a[44:47], v[156:159], v[60:63]\n"
    "ds_read_b128 v[188:191], v33 offset:4096\n"
    "ds_read_b64_tr_b16 a[0:1], v2 offset:25344\n"
    "v_mfma_f32_16x16x32_bf16 v[64:67], a[48:51], v[156:159], v[64:67]\n"
    "ds_read_b64_tr_b16 a[2:3], v2 offset:25856\n"
    "v_mfma_f32_16x16x32_bf16 v[68:71], a[52:55], v[156:159], v[68:71]\n"
    "ds_read_b64_tr_b16 a[4:5], v2 offset:25376\n"
    "ds_read_b64_tr_b16 a[6:7], v2 offset:25888\n"
    "v_mfma_f32_16x16x32_bf16 v[72:75], a[56:59], v[156:159], v[72:75]\n"
    "ds_read_b64_tr_b16 a[8:9], v2 offset:25408\n"
    "v_mfma_f32_16x16x32_bf16 v[76:79], a[60:63], v[156:159], v[76:79]\n"
    "ds_read_b64_tr_b16 a[10:11], v2 offset:25920\n"
    "ds_read_b64_tr_b16 a[12:13], v2 offset:25440\n"
    "v_mfma_f32_16x16x32_bf16 v[80:83], a[32:35], v[160:163], v[80:83]\n"
    "ds_read_b64_tr_b16 a[14:15], v2 offset:25952\n"
    "ds_read_b64_tr_b16 a[16:17], v2 offset:25472\n"
    "s_waitcnt lgkmcnt(9)\n"
    "s_waitcnt vmcnt(9)\n"
    "ds_add_u32 v4, v6\n"
    "s_add_u32 s46, s46, 1\n"
    "s_setprio 0\n"
    "v_mfma_f32_16x16x32_bf16 v[84:87], a[36:39], v[160:163], v[84:87]\n"
    "ds_read_b64_tr_b16 a[18:19], v2 offset:25984\n"
    "v_mfma_f32_16x16x32_bf16 v[88:91], a[40:43], v[160:163], v[88:91]\n"
    "ds_read_b64_tr_b16 a[20:21], v2 offset:25504\n"
    "ds_read_b64_tr_b16 a[22:23], v2 offset:26016\n"
    "s_bfe_u32 s54, %[inbox], 0x3000d\n"
    "s_lshl_b32 s54, s54, 17\n"
    "s_bfe_u32 s55, %[inbox], 0x30007\n"
    "s_lshl_b32 s55, s55, 14\n"
    "v_mfma_f32_16x16x32_bf16 v[92:95], a[44:47], v[160:163], v[92:95]\n"
    "ds_read_b64_tr_b16 a[24:25], v2 offset:25536\n"
    "s_add_u32 s54, s54, s55\n"
    "s_lshl_b32 s55, %[w], 11\n"
    "s_sub_u32 s54, s54, s55\n"
    "v_mfma_f32_16x16x32_bf16 v[96:99], a[48:51], v[160:163], v[96:99]\n"
    "ds_read_b64_tr_b16 a[26:27], v2 offset:26048\n"
    "s_waitcnt lgkmcnt(14)\n"
    "ds_read_b64_tr_b16 a[28:29], v2 offset:25568\n"
    "v_lshrrev_b32 v44, 15, v3\n"
    "v_lshlrev_b32 v44, 10, v44\n"
    "v_and_b32 v45, 0xf0, v3\n"
    "v_add3_u32 v44, v44, v45, s54\n"
    "v_mfma_f32_16x16x32_bf16 v[100:103], a[52:55], v[160:163], v[100:103]\n"
    "s_waitcnt lgkmcnt(14)\n"
    "ds_read_b64_tr_b16 a[30:31], v2 offset:26080\n"
    "v_mfma_f32_16x16x32_bf16 v[104:107], a[56:59], v[160:163], v[104:107]\n"
    "s_waitcnt lgkmcnt(14)\n"
    "ds_read_b32 v7, v5\n"
    "v_mfma_f32_16x16x32_bf16 v[108:111], a[60:63], v[160:163], v[108:111]\n"
    "s_add_u32 m0, %[lds], 158464\n"
    "s_nop 0\n"
    "global_load_lds_dwordx4 v44, %[wp2]\n"
    "v_add_u32 v44, 4096, v44\n"
    "v_mfma_f32_16x16x32_bf16 v[112:115], a[32:35], v[164:167], v[112:115]\n"
    "s_memtime s[54:55]\n"
    "s_memrealtime s[56:57]\n"
    "s_waitcnt lgkmcnt(0)\n"
    "s_mov_b32 s72, s54\n"
    "s_mov_b32 s73, s56\n"
    "v_mfma_f32_16x16x32_bf16 v[116:119], a[36:39], v[164:167], v[116:119]\n"
    "v_cmp_gt_u32 vcc, s46, v7\n"
    "v_mfma_f32_16x16x32_bf16 v[120:123], a[40:43], v[164:167], v[120:123]\n"
    "s_cbranch_vccnz Lfs_4_%=\n"
    "Lfb_3_%=:\n"
    "v_mfma_f32_16x16x32_bf16 v[124:127], a[44:47], v[164:167], v[124:127]\n"
    "s_add_u32 m0, s48, 76032\n"
    "s_add_u32 s76, s78, 384\n"
    "buffer_load_dwordx4 v13, s[68:71], s76 offen lds\n"
    "v_mfma_f32_16x16x32_bf16 v[128:131], a[48:51], v[164:167], v[128:131]\n"
    "v_mfma_f32_16x16x32_bf16 v[132:135], a[52:55], v[164:167], v[132:135]\n"
    "v_mfma_f32_16x16x32_bf16 v[136:139], a[56:59], v[164:167], v[136:139]\n"
    "s_add_u32 m0, s48, 77056\n"
    "s_add_u32 s77, s78, 8576\n"
    "buffer_load_dwordx4 v14, s[68:71], s77 offen lds\n"
    "v_mfma_f32_16x16x32_bf16 v[140:143], a[60:63], v[164:167], v[140:143]\n"
    "s_setprio 3\n"
    "v_mfma_f32_16x16x32_bf16 v[48:51], a[0:3], v[168:171], v[48:51]\n"
    "ds_read_b64_tr_b16 a[32:33], v2 offset:33792\n"
    "ds_read_b64_tr_b16 a[34:35], v2 offset:34304\n"
    "v_mfma_f32_16x16x32_bf16 v[52:55], a[4:7], v[168:171], v[52:55]\n"
    "s_add_u32 m0, s48, 78080\n"
    "s_add_u32 s76, s78, 16768\n"
    "buffer_load_dwordx4 v13, s[68:71], s76 offen lds\n"
    "ds_read_b64_tr_b16 a[36:37], v2 offset:33824\n"
    "v_mfma_f32_16x16x32_bf16 v[56:59], a[8:11], v[168:171], v[56:59]\n"
    "ds_read_b64_tr_b16 a[38:39], v2 offset:34336\n"
    "v_mfma_f32_16x16x32_bf16 v[60:63], a[12:15], v[168:171], v[60:63]\n"
    "ds_read_b64_tr_b16 a[40:41], v2 offset:33856\n"
    "v_mfma_f32_16x16x32_bf16 v[64:67], a[16:19], v[168:171], v[64:67]\n"
    "ds_read_b64_tr_b16 a[42:43], v2 offset:34368\n"
    "v_mfma_f32_16x16x32_bf16 v[68:71], a[20:23], v[168:171], v[68:71]\n"
    "s_add_u32 m0, s40, 16896\n"
    "s_add_u32 s44, s43, 8192\n"
    "buffer_load_dwordx4 v12, s[28:31], s43 offen lds\n"
    "ds_read_b64_tr_b16 a[44:45], v2 offset:33888\n"
    "v_mfma_f32_16x16x32_bf16 v[72:75], a[24:27], v[168:171], v[72:75]\n"
    "ds_read_b64_tr_b16 a[46:47], v2 offset:34400\n"
    "v_mfma_f32_16x16x32_bf16 v[76:79], a[28:31], v[168:171], v[76:79]\n"
    "ds_read_b64_tr_b16 a[48:49], v2 offset:33920\n"
    "ds_read_b64_tr_b16 a[50:51], v2 offset:34432\n"
    "v_mfma_f32_16x16x32_bf16 v[80:83], a[0:3], v[172:175], v[80:83]\n"
    "ds_read_b64_tr_b16 a[52:53], v2 offset:33952\n"
    "s_waitcnt vmcnt(9)\n"
    "ds_add_u32 v4, v6\n"
    "s_add_u32 s46, s46, 1\n"
    "s_setprio 0\n"
    "v_mfma_f32_16x16x32_bf16 v[84:87], a[4:7], v[172:175], v[84:87]\n"
    "s_add_u32 m0, s40, 17952\n"
    "s_add_u32 s43, s43, 262144\n"
    "buffer_load_dwordx4 v12, s[28:31], s44 offen lds\n"
    "ds_read_b64_tr_b16 a[54:55], v2 offset:34464\n"
    "v_mfma_f32_16x16x32_bf16 v[88:91], a[8:11], v[172:175], v[88:91]\n"
    "ds_read_b64_tr_b16 a[56:57], v2 offset:33984\n"
    "v_mfma_f32_16x16x32_bf16 v[92:95], a[12:15], v[172:175], v[92:95]\n"
    "ds_read_b64_tr_b16 a[58:59], v2 offset:34496\n"
    "v_mfma_f32_16x16x32_bf16 v[96:99], a[16:19], v[172:175], v[96:99]\n"
    "s_waitcnt lgkmcnt(14)\n"
    "ds_read_b64_tr_b16 a[60:61], v2 offset:34016\n"
    "v_mfma_f32_16x16x32_bf16 v[100:103], a[20:23], v[172:175], v[100:103]\n"
    "s_waitcnt lgkmcnt(14)\n"
    "ds_read_b64_tr_b16 a[62:63], v2 offset:34528\n"
    "v_mfma_f32_16x16x32_bf16 v[104:107], a[24:27], v[172:175], v[104:107]\n"
    "s_waitcnt lgkmcnt(14)\n"
    "ds_read_b32 v7, v5\n"
    "v_mfma_f32_16x16x32_bf16 v[108:111], a[28:31], v[172:175], v[108:111]\n"
    "v_mfma_f32_16x16x32_bf16 v[112:115], a[0:3], v[176:179], v[112:115]\n"
    "v_mfma_f32_16x16x32_bf16 v[116:119], a[4:7], v[176:179], v[116:119]\n"
    "s_waitcnt lgkmcnt(0)\n"
    "v_cmp_gt_u32 vcc, s46, v7\n"
    "v_mfma_f32_16x16x32_bf16 v[120:123], a[8:11], v[176:179], v[120:123]\n"
    "s_cbranch_vccnz Lfs_6_%=\n"
    "Lfb_5_%=:\n"
    "v_mfma_f32_16x16x32_bf16 v[124:127], a[12:15], v[176:179], v[124:127]\n"
    "s_add_u32 m0, s40, 25344\n"
    "s_add_u32 s44, s43, 8192\n"
    "buffer_load_dwordx4 v12, s[28:31], s43 offen lds\n"
    "v_mfma_f32_16x16x32_bf16 v[128:131], a[16:19], v[176:179], v[128:131]\n"
    "v_mfma_f32_16x16x32_bf16 v[132:135], a[20:23], v[176:179], v[132:135]\n"
    "v_mfma_f32_16x16x32_bf16 v[136:139], a[24:27], v[176:179], v[136:139]\n"
    "s_add_u32 m0, s40, 26400\n"
    "s_add_u32 s43, s43, 262144\n"
    "buffer_load_dwordx4 v12, s[28:31], s44 offen lds\n"
    "v_mfma_f32_16x16x32_bf16 v[140:143], a[28:31], v[176:179], v[140:143]\n"
    "s_setprio 3\n"
    "v_mfma_f32_16x16x32_bf16 v[48:51], a[32:35], v[180:183], v[48:51]\n"
    "ds_read_b128 v[144:147], v30 offset:0\n"
    "ds_read_b128 v[148:151], v30 offset:2048\n"
    "v_mfma_f32_16x16x32_bf16 v[52:55], a[36:39], v[180:183], v[52:55]\n"
    "ds_read_b128 v[152:155], v30 offset:4096\n"
    "ds_read_b128 v[156:159], v31 offset:0\n"
    "v_mfma_f32_16x16x32_bf16 v[56:59], a[40:43], v[180:183], v[56:59]\n"
    "ds_read_b128 v[160:163], v31 offset:2048\n"
    "v_mfma_f32_16x16x32_bf16 v[60:63], a[44:47], v[180:183], v[60:63]\n"
    "ds_read_b128 v[164:167], v31 offset:4096\n"
    "ds_read_b64_tr_b16 a[0:1], v2 offset:0\n"
    "v_mfma_f32_16x16x32_bf16 v[64:67], a[48:51], v[180:183], v[64:67]\n"
    "ds_read_b64_tr_b16 a[2:3], v2 offset:512\n"
    "v_mfma_f32_16x16x32_bf16 v[68:71], a[52:55], v[180:183], v[68:71]\n"
    "ds_read_b64_tr_b16 a[4:5], v2 offset:32\n"
    "ds_read_b64_tr_b16 a[6:7], v2 offset:544\n"
    "v_mfma_f32_16x16x32_bf16 v[72:75], a[56:59], v[180:183], v[72:75]\n"
    "ds_read_b64_tr_b16 a[8:9], v2 offset:64\n"
    "v_mfma_f32_16x16x32_bf16 v[76:79], a[60:63], v[180:183], v[76:79]\n"
    "ds_read_b64_tr_b16 a[10:11], v2 offset:576\n"
    "ds_read_b64_tr_b16 a[12:13], v2 offset:96\n"
    "v_mfma_f32_16x16x32_bf16 v[80:83], a[32:35], v[184:187], v[80:83]\n"
    "ds_read_b64_tr_b16 a[14:15], v2 offset:608\n"
    "ds_read_b64_tr_b16 a[16:17], v2 offset:128\n"
    "s_waitcnt lgkmcnt(9)\n"
    "s_waitcnt vmcnt(10)\n"
    "ds_add_u32 v4, v6\n"
    "s_add_u32 s46, s46, 1\n"
    "s_setprio 0\n"
    "v_mfma_f32_16x16x32_bf16 v[84:87], a[36:39], v[184:187], v[84:87]\n"
    "ds_read_b64_tr_b16 a[18:19], v2 offset:640\n"
    "v_mfma_f32_16x16x32_bf16 v[88:91], a[40:43], v[184:187], v[88:91]\n"
    "ds_read_b64_tr_b16 a[20:21], v2 offset:160\n"
    "ds_read_b64_tr_b16 a[22:23], v2 offset:672\n"
    "v_mfma_f32_16x16x32_bf16 v[92:95], a[44:47], v[184:187], v[92:95]\n"
    "ds_read_b64_tr_b16 a[24:25], v2 offset:192\n"
    "v_mfma_f32_16x16x32_bf16 v[96:99], a[48:51], v[184:187], v[96:99]\n"
    "ds_read_b64_tr_b16 a[26:27], v2 offset:704\n"
    "s_waitcnt lgkmcnt(14)\n"
    "ds_read_b64_tr_b16 a[28:29], v2 offset:224\n"
    "v_mfma_f32_16x16x32_bf16 v[100:103], a[52:55], v[184:187], v[100:103]\n"
    "s_waitcnt lgkmcnt(14)\n"
    "ds_read_b64_tr_b16 a[30:31], v2 offset:736\n"
    "v_mfma_f32_16x16x32_bf16 v[104:107], a[56:59], v[184:187], v[104:107]\n"
    "s_waitcnt lgkmcnt(14)\n"
    "ds_read_b32 v7, v5\n"
    "v_mfma_f32_16x16x32_bf16 v[108:111], a[60:63], v[184:187], v[108:111]\n"
    "s_add_u32 m0, %[lds], 158464\n"
    "s_nop 0\n"
    "global_load_lds_dwordx4 v44, %[wp2]\n"
    "v_add_u32 v44, 4096, v44\n"
    "v_mfma_f32_16x16x32_bf16 v[112:115], a[32:35], v[188:191], v[112:115]\n"
    "v_mfma_f32_16x16x32_bf16 v[116:119], a[36:39], v[188:191], v[116:119]\n"
    "s_waitcnt lgkmcnt(0)\n"
    "v_cmp_gt_u32 vcc, s46, v7\n"
    "v_mfma_f32_16x16x32_bf16 v[120:123], a[40:43], v[188:191], v[120:123]\n"
    "s_cbranch_vccnz Lfs_8_%=\n"
    "Lfb_7_%=:\n"
    "v_mfma_f32_16x16x32_bf16 v[124:127], a[44:47], v[188:191], v[124:127]\n"
    "s_add_u32 m0, s48, 0\n"
    "s_add_u32 s76, s78, 512\n"
    "buffer_load_dwordx4 v13, s[68:71], s76 offen lds\n"
    "v_mfma_f32_16x16x32_bf16 v[128:131], a[48:51], v[188:191], v[128:131]\n"
    "v_mfma_f32_16x16x32_bf16 v[132:135], a[52:55], v[188:191], v[132:135]\n"
    "v_mfma_f32_16x16x32_bf16 v[136:139], a[56:59], v[188:191], v[136:139]\n"
    "s_add_u32 m0, s48, 1024\n"
    "s_add_u32 s77, s78, 8704\n"
    "buffer_load_dwordx4 v14, s[68:71], s77 offen lds\n"
    "v_mfma_f32_16x16x32_bf16 v[140:143], a[60:63], v[188:191], v[140:143]\n"
    "s_setprio 3\n"
    "v_mfma_f32_16x16x32_bf16 v[48:51], a[0:3], v[144:147], v[48:51]\n"
    "ds_read_b64_tr_b16 a[32:33], v2 offset:42240\n"
    "ds_read_b64_tr_b16 a[34:35], v2 offset:42752\n"
    "v_mfma_f32_16x16x32_bf16 v[52:55], a[4:7], v[144:147], v[52:55]\n"
    "s_add_u32 m0, s48, 2048\n"
    "s_add_u32 s76, s78, 16896\n"
    "buffer_load_dwordx4 v13, s[68:71], s76 offen lds\n"
    "ds_read_b64_tr_b16 a[36:37], v2 offset:42272\n"
    "v_mfma_f32_16x16x32_bf16 v[56:59], a[8:11], v[144:147], v[56:59]\n"
    "ds_read_b64_tr_b16 a[38:39], v2 offset:42784\n"
    "v_mfma_f32_16x16x32_bf16 v[60:63], a[12:15], v[144:147], v[60:63]\n"
    "ds_read_b64_tr_b16 a[40:41], v2 offset:42304\n"
    "v_mfma_f32_16x16x32_bf16 v[64:67], a[16:19], v[144:147], v[64:67]\n"
    "ds_read_b64_tr_b16 a[42:43], v2 offset:42816\n"
    "v_mfma_f32_16x16x32_bf16 v[68:71], a[20:23], v[144:147], v[68:71]\n"
    "s_add_u32 m0, s40, 33792\n"
    "s_add_u32 s44, s43, 8192\n"
    "buffer_load_dwordx4 v12, s[28:31], s43 offen lds\n"
    "ds_read_b64_tr_b16 a[44:45], v2 offset:42336\n"
    "v_mfma_f32_16x16x32_bf16 v[72:75], a[24:27], v[144:147], v[72:75]\n"
    "ds_read_b64_tr_b16 a[46:47], v2 offset:42848\n"
    "v_mfma_f32_16x16x32_bf16 v[76:79], a[28:31], v[144:147], v[76:79]\n"
    "ds_read_b64_tr_b16 a[48:49], v2 offset:42368\n"
    "ds_read_b64_tr_b16 a[50:51], v2 offset:42880\n"
    "v_mfma_f32_16x16x32_bf16 v[80:83], a[0:3], v[148:151], v[80:83]\n"
    "ds_read_b64_tr_b16 a[52:53], v2 offset:42400\n"
    "s_waitcnt vmcnt(13)\n"
    "s_waitcnt vmcnt(9)\n"
    "ds_add_u32 v4, v6\n"
    "s_add_u32 s46, s46, 1\n"
    "s_setprio 0\n"
    "v_mfma_f32_16x16x32_bf16 v[84:87], a[4:7], v[148:151], v[84:87]\n"
    "s_add_u32 m0, s40, 34848\n"
    "s_add_u32 s43, s43, 262144\n"
    "buffer_load_dwordx4 v12, s[28:31], s44 offen lds\n"
    "ds_read_b64_tr_b16 a[54:55], v2 offset:42912\n"
    "v_mfma_f32_16x16x32_bf16 v[88:91], a[8:11], v[148:151], v[88:91]\n"
    "ds_read_b64_tr_b16 a[56:57], v2 offset:42432\n"
    "v_mfma_f32_16x16x32_bf16 v[92:95], a[12:15], v[148:151], v[92:95]\n"
    "ds_read_b64_tr_b16 a[58:59], v2 offset:42944\n"
    "v_mfma_f32_16x16x32_bf16 v[96:99], a[16:19], v[148:151], v[96:99]\n"
    "s_waitcnt lgkmcnt(14)\n"
    "ds_read_b64_tr_b16 a[60:61], v2 offset:42464\n"
    "v_mfma_f32_16x16x32_bf16 v[100:103], a[20:23], v[148:151], v[100:103]\n"
    "s_waitcnt lgkmcnt(14)\n"
    "ds_read_b64_tr_b16 a[62:63], v2 offset:42976\n"
    "v_mfma_f32_16x16x32_bf16 v[104:107], a[24:27], v[148:151], v[104:107]\n"
    "s_waitcnt lgkmcnt(14)\n"
    "ds_read_b32 v7, v5\n"
    "v_mfma_f32_16x16x32_bf16 v[108:111], a[28:31], v[148:151], v[108:111]\n"
    "v_mfma_f32_16x16x32_bf16 v[112:115], a[0:3], v[152:155], v[112:115]\n"
    "v_mfma_f32_16x16x32_bf16 v[116:119], a[4:7], v[152:155], v[116:119]\n"
    "s_waitcnt lgkmcnt(0)\n"
    "v_cmp_gt_u32 vcc, s46, v7\n"
    "v_mfma_f32_16x16x32_bf16 v[120:123], a[8:11], v[152:155], v[120:123]\n"
    "s_cbranch_vccnz Lfs_10_%=\n"
    "Lfb_9_%=:\n"
    "v_mfma_f32_16x16x32_bf16 v[124:127], a[12:15], v[152:155], v[124:127]\n"
    "s_add_u32 m0, s40, 0\n"
    "s_add_u32 s44, s43, 8192\n"
    "buffer_load_dwordx4 v12, s[28:31], s43 offen lds\n"
    "v_mfma_f32_16x16x32_bf16 v[128:131], a[16:19], v[152:155], v[128:131]\n"
    "v_mfma_f32_16x16x32_bf16 v[132:135], a[20:23], v[152:155], v[132:135]\n"
    "v_mfma_f32_16x16x32_bf16 v[136:139], a[24:27], v[152:155], v[136:139]\n"
    "s_add_u32 m0, s40, 1056\n"
    "s_add_u32 s43, s43, 262144\n"
    "buffer_load_dwordx4 v12, s[28:31], s44 offen lds\n"
    "v_mfma_f32_16x16x32_bf16 v[140:143], a[28:31], v[152:155], v[140:143]\n"
    "s_setprio 3\n"
    "v_mfma_f32_16x16x32_bf16 v[48:51], a[32:35], v[156:159], v[48:51]\n"
    "ds_read_b128 v[168:171], v32 offset:0\n"
    "ds_read_b128 v[172:175], v32 offset:2048\n"
    "v_mfma_f32_16x16x32_bf16 v[52:55], a[36:39], v[156:159], v[52:55]\n"
    "ds_read_b128 v[176:179], v32 offset:4096\n"
    "ds_read_b128 v[180:183], v33 offset:0\n"
    "v_mfma_f32_16x16x32_bf16 v[56:59], a[40:43], v[156:159], v[56:59]\n"
    "ds_read_b128 v[184:187], v33 offset:2048\n"
    "v_mfma_f32_16x16x32_bf16 v[60:63], a[44:47], v[156:159], v[60:63]\n"
    "ds_read_b128 v[188:191], v33 offset:4096\n"
    "ds_read_b64_tr_b16 a[0:1], v2 offset:8448\n"
    "v_mfma_f32_16x16x32_bf16 v[64:67], a[48:51], v[156:159], v[64:67]\n"
    "ds_read_b64_tr_b16 a[2:3], v2 offset:8960\n"
    "v_mfma_f32_16x16x32_bf16 v[68:71], a[52:55], v[156:159], v[68:71]\n"
    "ds_read_b64_tr_b16 a[4:5], v2 offset:8480\n"
    "ds_read_b64_tr_b16 a[6:7], v2 offset:8992\n"
    "v_mfma_f32_16x16x32_bf16 v[72:75], a[56:59], v[156:159], v[72:75]\n"
    "ds_read_b64_tr_b16 a[8:9], v2 offset:8512\n"
    "v_mfma_f32_16x16x32_bf16 v[76:79], a[60:63], v[156:159], v[76:79]\n"
    "ds_read_b64_tr_b16 a[10:11], v2 offset:9024\n"
    "ds_read_b64_tr_b16 a[12:13], v2 offset:8544\n"
    "v_mfma_f32_16x16x32_bf16 v[80:83], a[32:35], v[160:163], v[80:83]\n"
    "ds_read_b64_tr_b16 a[14:15], v2 offset:9056\n"
    "ds_read_b64_tr_b16 a[16:17], v2 offset:8576\n"
    "s_waitcnt lgkmcnt(9)\n"
    "s_waitcnt vmcnt(10)\n"
    "ds_add_u32 v4, v6\n"
    "s_add_u32 s46, s46, 1\n"
    "s_setprio 0\n"
    "v_mfma_f32_16x16x32_bf16 v[84:87], a[36:39], v[160:163], v[84:87]\n"
    "ds_read_b64_tr_b16 a[18:19], v2 offset:9088\n"
    "v_mfma_f32_16x16x32_bf16 v[88:91], a[40:43], v[160:163], v[88:91]\n"
    "ds_read_b64_tr_b16 a[20:21], v2 offset:8608\n"
    "ds_read_b64_tr_b16 a[22:23], v2 offset:9120\n"
    "v_mfma_f32_16x16x32_bf16 v[92:95], a[44:47], v[160:163], v[92:95]\n"
    "ds_read_b64_tr_b16 a[24:25], v2 offset:8640\n"
    "v_mfma_f32_16x16x32_bf16 v[96:99], a[48:51], v[160:163], v[96:99]\n"
    "ds_read_b64_tr_b16 a[26:27], v2 offset:9152\n"
    "s_waitcnt lgkmcnt(14)\n"
    "ds_read_b64_tr_b16 a[28:29], v2 offset:8672\n"
    "v_mfma_f32_16x16x32_bf16 v[100:103], a[52:55], v[160:163], v[100:103]\n"
    "s_waitcnt lgkmcnt(14)\n"
    "ds_read_b64_tr_b16 a[30:31], v2 offset:9184\n"
    "v_mfma_f32_16x16x32_bf16 v[104:107], a[56:59], v[160:163], v[104:107]\n"
    "s_waitcnt lgkmcnt(14)\n"
    "ds_read_b32 v7, v5\n"
    "v_mfma_f32_16x16x32_bf16 v[108:111], a[60:63], v[160:163], v[108:111]\n"
    "s_add_u32 m0, %[lds], 158464\n"
    "s_nop 0\n"
    "global_load_lds_dwordx4 v44, %[wp2]\n"
    "v_add_u32 v44, 4096, v44\n"
    "v_mfma_f32_16x16x32_bf16 v[112:115], a[32:35], v[164:167], v[112:115]\n"
    "v_mfma_f32_16x16x32_bf16 v[116:119], a[36:39], v[164:167], v[116:119]\n"
    "s_waitcnt lgkmcnt(0)\n"
    "v_cmp_gt_u32 vcc, s46, v7\n"
    "v_mfma_f32_16x16x32_bf16 v[120:123], a[40:43], v[164:167], v[120:123]\n"
    "s_cbranch_vccnz Lfs_12_%=\n"
    "Lfb_11_%=:\n"
    "v_mfma_f32_16x16x32_bf16 v[124:127], a[44:47], v[164:167], v[124:127]\n"
    "s_add_u32 m0, s48, 76032\n"
    "s_add_u32 s76, s78, 640\n"
    "buffer_load_dwordx4 v13, s[68:71], s76 offen lds\n"
    "v_mfma_f32_16x16x32_bf16 v[128:131], a[48:51], v[164:167], v[128:131]\n"
    "v_mfma_f32_16x16x32_bf16 v[132:135], a[52:55], v[164:167], v[132:135]\n"
    "v_mfma_f32_16x16x32_bf16 v[136:139], a[56:59], v[164:167], v[136:139]\n"
    "s_add_u32 m0, s48, 77056\n"
    "s_add_u32 s77, s78, 8832\n"
    "buffer_load_dwordx4 v14, s[68:71], s77 offen lds\n"
    "v_mfma_f32_16x16x32_bf16 v[140:143], a[60:63], v[164:167], v[140:143]\n"
    "s_setprio 3\n"
    "v_mfma_f32_16x16x32_bf16 v[48:51], a[0:3], v[168:171], v[48:51]\n"
    "ds_read_b64_tr_b16 a[32:33], v2 offset:16896\n"
    "ds_read_b64_tr_b16 a[34:35], v2 offset:17408\n"
    "v_mfma_f32_16x16x32_bf16 v[52:55], a[4:7], v[168:171], v[52:55]\n"
    "s_add_u32 m0, s48, 78080\n"
    "s_add_u32 s76, s78, 17024\n"
    "buffer_load_dwordx4 v13, s[68:71], s76 offen lds\n"
    "ds_read_b64_tr_b16 a[36:37], v2 offset:16928\n"
    "v_mfma_f32_16x16x32_bf16 v[56:59], a[8:11], v[168:171], v[56:59]\n"
    "ds_read_b64_tr_b16 a[38:39], v2 offset:17440\n"
    "v_mfma_f32_16x16x32_bf16 v[60:63], a[12:15], v[168:171], v[60:63]\n"
    "ds_read_b64_tr_b16 a[40:41], v2 offset:16960\n"
    "v_mfma_f32_16x16x32_bf16 v[64:67], a[16:19], v[168:171], v[64:67]\n"
    "ds_read_b64_tr_b16 a[42:43], v2 offset:17472\n"
    "v_mfma_f32_16x16x32_bf16 v[68:71], a[20:23], v[168:171], v[68:71]\n"
    "s_add_u32 m0, s40, 42240\n"
    "s_add_u32 s44, s43, 8192\n"
    "buffer_load_dwordx4 v12, s[28:31], s43 offen lds\n"
    "ds_read_b64_tr_b16 a[44:45], v2 offset:16992\n"
    "v_mfma_f32_16x16x32_bf16 v[72:75], a[24:27], v[168:171], v[72:75]\n"
    "ds_read_b64_tr_b16 a[46:47], v2 offset:17504\n"
    "v_mfma_f32_16x16x32_bf16 v[76:79], a[28:31], v[168:171], v[76:79]\n"
    "ds_read_b64_tr_b16 a[48:49], v2 offset:17024\n"
    "ds_read_b64_tr_b16 a[50:51], v2 offset:17536\n"
    "v_mfma_f32_16x16x32_bf16 v[80:83], a[0:3], v[172:175], v[80:83]\n"
    "ds_read_b64_tr_b16 a[52:53], v2 offset:17056\n"
    "s_waitcnt vmcnt(13)\n"
    "s_waitcnt vmcnt(9)\n"
    "ds_add_u32 v4, v6\n"
    "s_add_u32 s46, s46, 1\n"
    "s_setprio 0\n"
    "v_mfma_f32_16x16x32_bf16 v[84:87], a[4:7], v[172:175], v[84:87]\n"
    "s_add_u32 m0, s40, 43296\n"
    "s_add_u32 s43, s43, 262144\n"
    "buffer_load_dwordx4 v12, s[28:31], s44 offen lds\n"
    "ds_read_b64_tr_b16 a[54:55], v2 offset:17568\n"
    "s_memtime s[54:55]\n"
    "s_memrealtime s[56:57]\n"
    "s_waitcnt lgkmcnt(0)\n"
    "s_sub_u32 s54, s54, s72\n"
    "s_sub_u32 s56, s56, s73\n"
    "v_cvt_f32_u32 v45, s56\n"
    "v_cvt_f32_u32 v47, s54\n"
    "v_mfma_f32_16x16x32_bf16 v[88:91], a[8:11], v[172:175], v[88:91]\n"
    "ds_read_b64_tr_b16 a[56:57], v2 offset:17088\n"
    "v_rcp_f32 v45, v45\n"
    "s_and_b32 s54, %[inbox], 0x380\n"
    "s_lshl_b32 s54, s54, 4\n"
    "s_lshr_b32 s55, %[inbox], 7\n"
    "s_and_b32 s55, s55, 0x7f8\n"
    "s_or_b32 s54, s54, s55\n"
    "s_add_u32 s54, s54, 393216\n"
    "v_mul_f32 v47, v47, v45\n"
    "v_mov_b32 v45, s54\n"
    "v_mov_b32 v46, s80\n"
    "v_mfma_f32_16x16x32_bf16 v[92:95], a[12:15], v[172:175], v[92:95]\n"
    "ds_read_b64_tr_b16 a[58:59], v2 offset:17600\n"
    "v_readfirstlane_b32 s72, v47\n"
    "s_mov_b64 exec, 1\n"
    "global_store_dwordx2 v45, v[46:47], %[ws] sc1\n"
    "s_mov_b64 exec, -1\n"
    "v_mfma_f32_16x16x32_bf16 v[96:99], a[16:19], v[172:175], v[96:99]\n"
    "ds_read_b64_tr_b16 a[60:61], v2 offset:17120\n"
    "v_mfma_f32_16x16x32_bf16 v[100:103], a[20:23], v[172:175], v[100:103]\n"
    "ds_read_b64_tr_b16 a[62:63], v2 offset:17632\n"
    "v_mfma_f32_16x16x32_bf16 v[104:107], a[24:27], v[172:175], v[104:107]\n"
    "ds_read_b32 v7, v5\n"
    "v_mfma_f32_16x16x32_bf16 v[108:111], a[28:31], v[172:175], v[108:111]\n"
    "v_mfma_f32_16x16x32_bf16 v[112:115], a[0:3], v[176:179], v[112:115]\n"
    "v_mfma_f32_16x16x32_bf16 v[116:119], a[4:7], v[176:179], v[116:119]\n"
    "s_waitcnt lgkmcnt(0)\n"
    "v_cmp_gt_u32 vcc, s46, v7\n"
    "v_mfma_f32_16x16x32_bf16 v[120:123], a[8:11], v[176:179], v[120:123]\n"
    "s_cbranch_vccnz Lfs_14_%=\n"
    "Lfb_13_%=:\n"
    "v_mfma_f32_16x16x32_bf16 v[124:127], a[12:15], v[176:179], v[124:127]\n"
    "s_add_u32 m0, s40, 8448\n"
    "s_add_u32 s44, s43, 8192\n"
    "buffer_load_dwordx4 v12, s[28:31], s43 offen lds\n"
    "v_mfma_f32_16x16x32_bf16 v[128:131], a[16:19], v[176:179], v[128:131]\n"
    "v_mfma_f32_16x16x32_bf16 v[132:135], a[20:23], v[176:179], v[132:135]\n"
    "v_mfma_f32_16x16x32_bf16 v[136:139], a[24:27], v[176:179], v[136:139]\n"
    "s_add_u32 m0, s40, 9504\n"
    "s_add_u32 s43, s43, 262144\n"
    "buffer_load_dwordx4 v12, s[28:31], s44 offen lds\n"
    "v_mfma_f32_16x16x32_bf16 v[140:143], a[28:31], v[176:179], v[140:143]\n"
    "s_setprio 3\n"
    "v_mfma_f32_16x16x32_bf16 v[48:51], a[32:35], v[180:183], v[48:51]\n"
    "ds_read_b128 v[144:147], v30 offset:0\n"
    "ds_read_b128 v[148:151], v30 offset:2048\n"
    "v_mfma_f32_16x16x32_bf16 v[52:55], a[36:39], v[180:183], v[52:55]\n"
    "ds_read_b128 v[152:155], v30 offset:4096\n"
    "ds_read_b128 v[156:159], v31 offset:0\n"
    "v_mfma_f32_16x16x32_bf16 v[56:59], a[40:43], v[180:183], v[56:59]\n"
    "ds_read_b128 v[160:163], v31 offset:2048\n"
    "v_mfma_f32_16x16x32_bf16 v[60:63], a[44:47], v[180:183], v[60:63]\n"
    "ds_read_b128 v[164:167], v31 offset:4096\n"
    "ds_read_b64_tr_b16 a[0:1], v2 offset:25344\n"
    "v_mfma_f32_16x16x32_bf16 v[64:67], a[48:51], v[180:183], v[64:67]\n"
    "ds_read_b64_tr_b16 a[2:3], v2 offset:25856\n"
    "v_mfma_f32_16x16x32_bf16 v[68:71], a[52:55], v[180:183], v[68:71]\n"
    "ds_read_b64_tr_b16 a[4:5], v2 offset:25376\n"
    "ds_read_b64_tr_b16 a[6:7], v2 offset:25888\n"
    "v_mfma_f32_16x16x32_bf16 v[72:75], a[56:59], v[180:183], v[72:75]\n"
    "ds_read_b64_tr_b16 a[8:9], v2 offset:25408\n"
    "v_mfma_f32_16x16x32_bf16 v[76:79], a[60:63], v[180:183], v[76:79]\n"
    "ds_read_b64_tr_b16 a[10:11], v2 offset:25920\n"
    "ds_read_b64_tr_b16 a[12:13], v2 offset:25440\n"
    "v_mfma_f32_16x16x32_bf16 v[80:83], a[32:35], v[184:187], v[80:83]\n"
    "ds_read_b64_tr_b16 a[14:15], v2 offset:25952\n"
    "ds_read_b64_tr_b16 a[16:17], v2 offset:25472\n"
    "s_waitcnt lgkmcnt(9)\n"
    "s_waitcnt vmcnt(11)\n"
    "ds_add_u32 v4, v6\n"
    "s_add_u32 s46, s46, 1\n"
    "s_setprio 0\n"
    "v_mfma_f32_16x16x32_bf16 v[84:87], a[36:39], v[184:187], v[84:87]\n"
    "ds_read_b64_tr_b16 a[18:19], v2 offset:25984\n"
    "v_mfma_f32_16x16x32_bf16 v[88:91], a[40:43], v[184:187], v[88:91]\n"
    "ds_read_b64_tr_b16 a[20:21], v2 offset:25504\n"
    "ds_read_b64_tr_b16 a[22:23], v2 offset:26016\n"
    "v_mfma_f32_16x16x32_bf16 v[92:95], a[44:47], v[184:187], v[92:95]\n"
    "ds_read_b64_tr_b16 a[24:25], v2 offset:25536\n"
    "v_mfma_f32_16x16x32_bf16 v[96:99], a[48:51], v[184:187], v[96:99]\n"
    "ds_read_b64_tr_b16 a[26:27], v2 offset:26048\n"
    "s_waitcnt lgkmcnt(14)\n"
    "ds_read_b64_tr_b16 a[28:29], v2 offset:25568\n"
    "v_mfma_f32_16x16x32_bf16 v[100:103], a[52:55], v[184:187], v[100:103]\n"
    "s_waitcnt lgkmcnt(14)\n"
    "ds_read_b64_tr_b16 a[30:31], v2 offset:26080\n"
    "v_mfma_f32_16x16x32_bf16 v[104:107], a[56:59], v[184:187], v[104:107]\n"
    "s_waitcnt lgkmcnt(14)\n"
    "ds_read_b32 v7, v5\n"
    "v_mfma_f32_16x16x32_bf16 v[108:111], a[60:63], v[184:187], v[108:111]\n"
    "s_add_u32 m0, %[lds], 158464\n"
    "s_nop 0\n"
    "global_load_lds_dwordx4 v44, %[wp2]\n"
    "v_add_u32 v44, 4096, v44\n"
    "v_mfma_f32_16x16x32_bf16 v[112:115], a[32:35], v[188:191], v[112:115]\n"
    "v_mfma_f32_16x16x32_bf16 v[116:119], a[36:39], v[188:191], v[116:119]\n"
    "s_waitcnt lgkmcnt(0)\n"
    "v_cmp_gt_u32 vcc, s46, v7\n"
    "v_mfma_f32_16x16x32_bf16 v[120:123], a[40:43], v[188:191], v[120:123]\n"
    "s_cbranch_vccnz Lfs_16_%=\n"
    "Lfb_15_%=:\n"
    "v_mfma_f32_16x16x32_bf16 v[124:127], a[44:47], v[188:191], v[124:127]\n"
    "s_add_u32 m0, s48, 0\n"
    "s_add_u32 s76, s78, 768\n"
    "buffer_load_dwordx4 v13, s[68:71], s76 offen lds\n"
    "v_mfma_f32_16x16x32_bf16 v[128:131], a[48:51], v[188:191], v[128:131]\n"
    "v_mfma_f32_16x16x32_bf16 v[132:135], a[52:55], v[188:191], v[132:135]\n"
    "v_mfma_f32_16x16x32_bf16 v[136:139], a[56:59], v[188:191], v[136:139]\n"
    "s_add_u32 m0, s48, 1024\n"
    "s_add_u32 s77, s78, 8960\n"
    "buffer_load_dwordx4 v14, s[68:71], s77 offen lds\n"
    "v_mfma_f32_16x16x32_bf16 v[140:143], a[60:63], v[188:191], v[140:143]\n"
    "s_setprio 3\n"
    "v_mfma_f32_16x16x32_bf16 v[48:51], a[0:3], v[144:147], v[48:51]\n"
    "ds_read_b64_tr_b16 a[32:33], v2 offset:33792\n"
    "ds_read_b64_tr_b16 a[34:35], v2 offset:34304\n"
    "v_mfma_f32_16x16x32_bf16 v[52:55], a[4:7], v[144:147], v[52:55]\n"
    "s_add_u32 m0, s48, 2048\n"
    "s_add_u32 s76, s78, 17152\n"
    "buffer_load_dwordx4 v13, s[68:71], s76 offen lds\n"
    "ds_read_b64_tr_b16 a[36:37], v2 offset:33824\n"
    "v_mfma_f32_16x16x32_bf16 v[56:59], a[8:11], v[144:147], v[56:59]\n"
    "ds_read_b64_tr_b16 a[38:39], v2 offset:34336\n"
    "v_mfma_f32_16x16x32_bf16 v[60:63], a[12:15], v[144:147], v[60:63]\n"
    "ds_read_b64_tr_b16 a[40:41], v2 offset:33856\n"
    "v_mfma_f32_16x16x32_bf16 v[64:67], a[16:19], v[144:147], v[64:67]\n"
    "ds_read_b64_tr_b16 a[42:43], v2 offset:34368\n"
    "v_mfma_f32_16x16x32_bf16 v[68:71], a[20:23], v[144:147], v[68:71]\n"
    "s_add_u32 m0, s40, 16896\n"
    "s_add_u32 s44, s43, 8192\n"
    "buffer_load_dwordx4 v12, s[28:31], s43 offen lds\n"
    "ds_read_b64_tr_b16 a[44:45], v2 offset:33888\n"
    "v_mfma_f32_16x16x32_bf16 v[72:75], a[24:27], v[144:147], v[72:75]\n"
    "ds_read_b64_tr_b16 a[46:47], v2 offset:34400\n"
    "v_mfma_f32_16x16x32_bf16 v[76:79], a[28:31], v[144:147], v[76:79]\n"
    "ds_read_b64_tr_b16 a[48:49], v2 offset:33920\n"
    "ds_read_b64_tr_b16 a[50:51], v2 offset:34432\n"
    "v_mfma_f32_16x16x32_bf16 v[80:83], a[0:3], v[148:151], v[80:83]\n"
    "ds_read_b64_tr_b16 a[52:53], v2 offset:33952\n"
    "s_waitcnt vmcnt(14)\n"
    "s_waitcnt vmcnt(10)\n"
    "ds_add_u32 v4, v6\n"
    "s_add_u32 s46, s46, 1\n"
    "s_setprio 0\n"
    "v_mfma_f32_16x16x32_bf16 v[84:87], a[4:7], v[148:151], v[84:87]\n"
    "s_add_u32 m0, s40, 17952\n"
    "s_add_u32 s43, s43, 262144\n"
    "buffer_load_dwordx4 v12, s[28:31], s44 offen lds\n"
    "ds_read_b64_tr_b16 a[54:55], v2 offset:34464\n"
    "v_mfma_f32_16x16x32_bf16 v[88:91], a[8:11], v[148:151], v[88:91]\n"
    "ds_read_b64_tr_b16 a[56:57], v2 offset:33984\n"
    "v_mfma_f32_16x16x32_bf16 v[92:95], a[12:15], v[148:151], v[92:95]\n"
    "ds_read_b64_tr_b16 a[58:59], v2 offset:34496\n"
    "v_mfma_f32_16x16x32_bf16 v[96:99], a[16:19], v[148:151], v[96:99]\n"
    "s_waitcnt lgkmcnt(14)\n"
    "ds_read_b64_tr_b16 a[60:61], v2 offset:34016\n"
    "v_mfma_f32_16x16x32_bf16 v[100:103], a[20:23], v[148:151], v[100:103]\n"
    "s_waitcnt lgkmcnt(14)\n"
    "ds_read_b64_tr_b16 a[62:63], v2 offset:34528\n"
    "v_mfma_f32_16x16x32_bf16 v[104:107], a[24:27], v[148:151], v[104:107]\n"
    "s_waitcnt lgkmcnt(14)\n"
    "ds_read_b32 v7, v5\n"
    "v_mfma_f32_16x16x32_bf16 v[108:111], a[28:31], v[148:151], v[108:111]\n"
    "v_mfma_f32_16x16x32_bf16 v[112:115], a[0:3], v[152:155], v[112:115]\n"
    "v_mfma_f32_16x16x32_bf16 v[116:119], a[4:7], v[152:155], v[116:119]\n"
    "s_waitcnt lgkmcnt(0)\n"
    "v_cmp_gt_u32 vcc, s46, v7\n"
    "v_mfma_f32_16x16x32_bf16 v[120:123], a[8:11], v[152:155], v[120:123]\n"
    "s_cbranch_vccnz Lfs_18_%=\n"
    "Lfb_17_%=:\n"
    "v_mfma_f32_16x16x32_bf16 v[124:127], a[12:15], v[152:155], v[124:127]\n"
    "s_add_u32 m0, s40, 25344\n"
    "s_add_u32 s44, s43, 8192\n"
    "buffer_load_dwordx4 v12, s[28:31], s43 offen lds\n"
    "v_mfma_f32_16x16x32_bf16 v[128:131], a[16:19], v[152:155], v[128:131]\n"
    "v_mfma_f32_16x16x32_bf16 v[132:135], a[20:23], v[152:155], v[132:135]\n"
    "v_mfma_f32_16x16x32_bf16 v[136:139], a[24:27], v[152:155], v[136:139]\n"
    "s_add_u32 m0, s40, 26400\n"
    "s_add_u32 s43, s43, 262144\n"
    "buffer_load_dwordx4 v12, s[28:31], s44 offen lds\n"
    "v_mfma_f32_16x16x32_bf16 v[140:143], a[28:31], v[152:155], v[140:143]\n"
    "s_setprio 3\n"
    "v_mfma_f32_16x16x32_bf16 v[48:51], a[32:35], v[156:159], v[48:51]\n"
    "ds_read_b128 v[168:171], v32 offset:0\n"
    "ds_read_b128 v[172:175], v32 offset:2048\n"
    "v_mfma_f32_16x16x32_bf16 v[52:55], a[36:39], v[156:159], v[52:55]\n"
    "ds_read_b128 v[176:179], v32 offset:4096\n"
    "ds_read_b128 v[180:183], v33 offset:0\n"
    "v_mfma_f32_16x16x32_bf16 v[56:59], a[40:43], v[156:159], v[56:59]\n"
    "ds_read_b128 v[184:187], v33 offset:2048\n"
    "v_mfma_f32_16x16x32_bf16 v[60:63], a[44:47], v[156:159], v[60:63]\n"
    "ds_read_b128 v[188:191], v33 offset:4096\n"
    "ds_read_b64_tr_b16 a[0:1], v2 offset:0\n"
    "v_mfma_f32_16x16x32_bf16 v[64:67], a[48:51], v[156:159], v[64:67]\n"
    "ds_read_b64_tr_b16 a[2:3], v2 offset:512\n"
    "v_mfma_f32_16x16x32_bf16 v[68:71], a[52:55], v[156:159], v[68:71]\n"
    "ds_read_b64_tr_b16 a[4:5], v2 offset:32\n"
    "ds_read_b64_tr_b16 a[6:7], v2 offset:544\n"
    "v_mfma_f32_16x16x32_bf16 v[72:75], a[56:59], v[156:159], v[72:75]\n"
    "ds_read_b64_tr_b16 a[8:9], v2 offset:64\n"
    "v_mfma_f32_16x16x32_bf16 v[76:79], a[60:63], v[156:159], v[76:79]\n"
    "ds_read_b64_tr_b16 a[10:11], v2 offset:576\n"
    "ds_read_b64_tr_b16 a[12:13], v2 offset:96\n"
    "v_mfma_f32_16x16x32_bf16 v[80:83], a[32:35], v[160:163], v[80:83]\n"
    "ds_read_b64_tr_b16 a[14:15], v2 offset:608\n"
    "ds_read_b64_tr_b16 a[16:17], v2 offset:128\n"
    "s_waitcnt lgkmcnt(9)\n"
    "s_waitcnt vmcnt(11)\n"
    "ds_add_u32 v4, v6\n"
    "s_add_u32 s46, s46, 1\n"
    "s_setprio 0\n"
    "v_mfma_f32_16x16x32_bf16 v[84:87], a[36:39], v[160:163], v[84:87]\n"
    "ds_read_b64_tr_b16 a[18:19], v2 offset:640\n"
    "v_mfma_f32_16x16x32_bf16 v[88:91], a[40:43], v[160:163], v[88:91]\n"
    "ds_read_b64_tr_b16 a[20:21], v2 offset:160\n"
    "ds_read_b64_tr_b16 a[22:23], v2 offset:672\n"
    "v_mfma_f32_16x16x32_bf16 v[92:95], a[44:47], v[160:163], v[92:95]\n"
    "ds_read_b64_tr_b16 a[24:25], v2 offset:192\n"
    "v_mfma_f32_16x16x32_bf16 v[96:99], a[48:51], v[160:163], v[96:99]\n"
    "ds_read_b64_tr_b16 a[26:27], v2 offset:704\n"
    "s_waitcnt lgkmcnt(14)\n"
    "ds_read_b64_tr_b16 a[28:29], v2 offset:224\n"
    "v_mfma_f32_16x16x32_bf16 v[100:103], a[52:55], v[160:163], v[100:103]\n"
    "s_waitcnt lgkmcnt(14)\n"
    "ds_read_b64_tr_b16 a[30:31], v2 offset:736\n"
    "v_mfma_f32_16x16x32_bf16 v[104:107], a[56:59], v[160:163], v[104:107]\n"
    "s_waitcnt lgkmcnt(14)\n"
    "ds_read_b32 v7, v5\n"
    "v_mfma_f32_16x16x32_bf16 v[108:111], a[60:63], v[160:163], v[108:111]\n"
    "v_mfma_f32_16x16x32_bf16 v[112:115], a[32:35], v[164:167], v[112:115]\n"
    "v_mfma_f32_16x16x32_bf16 v[116:119], a[36:39], v[164:167], v[116:119]\n"
    "s_waitcnt lgkmcnt(0)\n"
    "v_cmp_gt_u32 vcc, s46, v7\n"
    "v_mfma_f32_16x16x32_bf16 v[120:123], a[40:43], v[164:167], v[120:123]\n"
    "s_cbranch_vccnz Lfs_20_%=\n"
    "Lfb_19_%=:\n"
    "v_mfma_f32_16x16x32_bf16 v[124:127], a[44:47], v[164:167], v[124:127]\n"
    "s_add_u32 m0, s48, 76032\n"
    "s_add_u32 s76, s78, 896\n"
    "buffer_load_dwordx4 v13, s[68:71], s76 offen lds\n"
    "v_mfma_f32_16x16x32_bf16 v[128:131], a[48:51], v[164:167], v[128:131]\n"
    "v_mfma_f32_16x16x32_bf16 v[132:135], a[52:55], v[164:167], v[132:135]\n"
    "v_mfma_f32_16x16x32_bf16 v[136:139], a[56:59], v[164:167], v[136:139]\n"
    "s_add_u32 m0, s48, 77056\n"
    "s_add_u32 s77, s78, 9088\n"
    "buffer_load_dwordx4 v14, s[68:71], s77 offen lds\n"
    "v_mfma_f32_16x16x32_bf16 v[140:143], a[60:63], v[164:167], v[140:143]\n"
    "s_setprio 3\n"
    "v_mfma_f32_16x16x32_bf16 v[48:51], a[0:3], v[168:171], v[48:51]\n"
    "ds_read_b64_tr_b16 a[32:33], v2 offset:42240\n"
    "ds_read_b64_tr_b16 a[34:35], v2 offset:42752\n"
    "v_mfma_f32_16x16x32_bf16 v[52:55], a[4:7], v[168:171], v[52:55]\n"
    "s_add_u32 m0, s48, 78080\n"
    "s_add_u32 s76, s78, 17280\n"
    "buffer_load_dwordx4 v13, s[68:71], s76 offen lds\n"
    "ds_read_b64_tr_b16 a[36:37], v2 offset:42272\n"
    "v_mfma_f32_16x16x32_bf16 v[56:59], a[8:11], v[168:171], v[56:59]\n"
    "ds_read_b64_tr_b16 a[38:39], v2 offset:42784\n"
    "v_mfma_f32_16x16x32_bf16 v[60:63], a[12:15], v[168:171], v[60:63]\n"
    "ds_read_b64_tr_b16 a[40:41], v2 offset:42304\n"
    "v_mfma_f32_16x16x32_bf16 v[64:67], a[16:19], v[168:171], v[64:67]\n"
    "ds_read_b64_tr_b16 a[42:43], v2 offset:42816\n"
    "v_mfma_f32_16x16x32_bf16 v[68:71], a[20:23], v[168:171], v[68:71]\n"
    "s_add_u32 m0, s40, 33792\n"
    "s_add_u32 s44, s43, 8192\n"
    "buffer_load_dwordx4 v12, s[28:31], s43 offen lds\n"
    "ds_read_b64_tr_b16 a[44:45], v2 offset:42336\n"
    "v_mfma_f32_16x16x32_bf16 v[72:75], a[24:27], v[168:171], v[72:75]\n"
    "ds_read_b64_tr_b16 a[46:47], v2 offset:42848\n"
    "v_mfma_f32_16x16x32_bf16 v[76:79], a[28:31], v[168:171], v[76:79]\n"
    "ds_read_b64_tr_b16 a[48:49], v2 offset:42368\n"
    "ds_read_b64_tr_b16 a[50:51], v2 offset:42880\n"
    "v_mfma_f32_16x16x32_bf16 v[80:83], a[0:3], v[172:175], v[80:83]\n"
    "ds_read_b64_tr_b16 a[52:53], v2 offset:42400\n"
    "s_waitcnt vmcnt(12)\n"
    "s_waitcnt vmcnt(8)\n"
    "ds_add_u32 v4, v6\n"
    "s_add_u32 s46, s46, 1\n"
    "s_setprio 0\n"
    "v_mfma_f32_16x16x32_bf16 v[84:87], a[4:7], v[172:175], v[84:87]\n"
    "s_add_u32 m0, s40, 34848\n"
    "s_add_u32 s43, s43, 262144\n"
    "buffer_load_dwordx4 v12, s[28:31], s44 offen lds\n"
    "ds_read_b64_tr_b16 a[54:55], v2 offset:42912\n"
    "v_and_b32 v42, 0x380f0, v3\n"
    "v_lshrrev_b32 v42, 2, v42\n"
    "v_min_u32 v42, 60, v42\n"
    "s_mov_b32 s55, 124061987\n"
    "v_mfma_f32_16x16x32_bf16 v[88:91], a[8:11], v[172:175], v[88:91]\n"
    "ds_read_b64_tr_b16 a[56:57], v2 offset:42432\n"
    "v_cmp_gt_u32 vcc, 32, v42\n"
    "s_cmp_eq_u32 %[team], 0\n"
    "s_cmov_b32 s55, 124059939\n"
    "v_mov_b32 v40, s55\n"
    "v_mfma_f32_16x16x32_bf16 v[92:95], a[12:15], v[172:175], v[92:95]\n"
    "ds_read_b64_tr_b16 a[58:59], v2 offset:42944\n"
    "v_mov_b32 v41, 90682145\n"
    "v_cndmask_b32 v40, v40, v41, vcc\n"
    "v_lshrrev_b32 v40, v42, v40\n"
    "v_add_u32 v36, %[inbox], v42\n"
    "v_mfma_f32_16x16x32_bf16 v[96:99], a[16:19], v[172:175], v[96:99]\n"
    "s_waitcnt lgkmcnt(14)\n"
    "ds_read_b64_tr_b16 a[60:61], v2 offset:42464\n"
    "v_bfe_u32 v41, v40, 2, 1\n"
    "v_mul_u32_u24 v41, 6, v41\n"
    "v_sub_u32 v41, 16, v41\n"
    "v_and_b32 v43, 3, v40\n"
    "v_mfma_f32_16x16x32_bf16 v[100:103], a[20:23], v[172:175], v[100:103]\n"
    "s_waitcnt lgkmcnt(14)\n"
    "ds_read_b64_tr_b16 a[62:63], v2 offset:42976\n"
    "v_lshlrev_b32 v43, v41, v43\n"
    "v_add_u32 v43, v43, v36\n"
    "v_lshlrev_b32 v41, v41, 3\n"
    "v_bfi_b32 v37, v41, v43, v36\n"
    "v_mfma_f32_16x16x32_bf16 v[104:107], a[24:27], v[172:175], v[104:107]\n"
    "s_waitcnt lgkmcnt(14)\n"
    "ds_read_b32 v7, v5\n"
    "v_bfe_u32 v40, v40, 3, 1\n"
    "v_mul_u32_u24 v40, 0x204, v40\n"
    "v_xor_b32 v37, 32, v37\n"
    "v_xor_b32 v37, v37, v40\n"
    "v_mfma_f32_16x16x32_bf16 v[108:111], a[28:31], v[172:175], v[108:111]\n"
    "v_mov_b32 v39, 0xffff0000\n"
    "v_mov_b32 v40, 0xffff\n"
    "v_cndmask_b32 v39, v39, v40, vcc\n"
    "v_mfma_f32_16x16x32_bf16 v[112:115], a[0:3], v[176:179], v[112:115]\n"
    "global_load_dword v40, v36, %[ws] sc1\n"
    "global_load_dword v41, v37, %[ws] sc1\n"
    "v_mfma_f32_16x16x32_bf16 v[116:119], a[4:7], v[176:179], v[116:119]\n"
    "s_waitcnt lgkmcnt(0)\n"
    "v_cmp_gt_u32 vcc, s46, v7\n"
    "v_mfma_f32_16x16x32_bf16 v[120:123], a[8:11], v[176:179], v[120:123]\n"
    "s_cbranch_vccnz Lfs_22_%=\n"
    "Lfb_21_%=:\n"
    "v_mfma_f32_16x16x32_bf16 v[124:127], a[12:15], v[176:179], v[124:127]\n"
    "s_add_u32 m0, s40, 0\n"
    "s_xor_b32 s77, s41, 0x2000\n"
    "buffer_load_dwordx4 v3, s[20:23], s41 offen nt lds\n"
    "v_mfma_f32_16x16x32_bf16 v[128:131], a[16:19], v[176:179], v[128:131]\n"
    "v_mfma_f32_16x16x32_bf16 v[132:135], a[20:23], v[176:179], v[132:135]\n"
    "v_mfma_f32_16x16x32_bf16 v[136:139], a[24:27], v[176:179], v[136:139]\n"
    "s_add_u32 m0, m0, 1056\n"
    "s_add_u32 s41, s41, 107520\n"
    "buffer_load_dwordx4 v3, s[20:23], s77 offen nt lds\n"
    "s_and_b32 s41, s41, 0x25cff\n"
    "v_mfma_f32_16x16x32_bf16 v[140:143], a[28:31], v[176:179], v[140:143]\n"
    "s_setprio 3\n"
    "v_mfma_f32_16x16x32_bf16 v[48:51], a[32:35], v[180:183], v[48:51]\n"
    "ds_read_b128 v[144:147], v30 offset:0\n"
    "ds_read_b128 v[148:151], v30 offset:2048\n"
    "v_mfma_f32_16x16x32_bf16 v[52:55], a[36:39], v[180:183], v[52:55]\n"
    "ds_read_b128 v[152:155], v30 offset:4096\n"
    "ds_read_b128 v[156:159], v31 offset:0\n"
    "v_mfma_f32_16x16x32_bf16 v[56:59], a[40:43], v[180:183], v[56:59]\n"
    "ds_read_b128 v[160:163], v31 offset:2048\n"
    "v_mfma_f32_16x16x32_bf16 v[60:63], a[44:47], v[180:183], v[60:63]\n"
    "ds_read_b128 v[164:167], v31 offset:4096\n"
    "ds_read_b64_tr_b16 a[0:1], v2 offset:8448\n"
    "v_mfma_f32_16x16x32_bf16 v[64:67], a[48:51], v[180:183], v[64:67]\n"
    "ds_read_b64_tr_b16 a[2:3], v2 offset:8960\n"
    "v_mfma_f32_16x16x32_bf16 v[68:71], a[52:55], v[180:183], v[68:71]\n"
    "ds_read_b64_tr_b16 a[4:5], v2 offset:8480\n"
    "ds_read_b64_tr_b16 a[6:7], v2 offset:8992\n"
    "v_mfma_f32_16x16x32_bf16 v[72:75], a[56:59], v[180:183], v[72:75]\n"
    "ds_read_b64_tr_b16 a[8:9], v2 offset:8512\n"
    "v_mfma_f32_16x16x32_bf16 v[76:79], a[60:63], v[180:183], v[76:79]\n"
    "ds_read_b64_tr_b16 a[10:11], v2 offset:9024\n"
    "ds_read_b64_tr_b16 a[12:13], v2 offset:8544\n"
    "v_mfma_f32_16x16x32_bf16 v[80:83], a[32:35], v[184:187], v[80:83]\n"
    "ds_read_b64_tr_b16 a[14:15], v2 offset:9056\n"
    "ds_read_b64_tr_b16 a[16:17], v2 offset:8576\n"
    "s_waitcnt lgkmcnt(9)\n"
    "s_waitcnt vmcnt(11)\n"
    "ds_add_u32 v4, v6\n"
    "s_add_u32 s46, s46, 1\n"
    "s_setprio 0\n"
    "v_mfma_f32_16x16x32_bf16 v[84:87], a[36:39], v[184:187], v[84:87]\n"
    "ds_read_b64_tr_b16 a[18:19], v2 offset:9088\n"
    "v_mfma_f32_16x16x32_bf16 v[88:91], a[40:43], v[184:187], v[88:91]\n"
    "ds_read_b64_tr_b16 a[20:21], v2 offset:8608\n"
    "ds_read_b64_tr_b16 a[22:23], v2 offset:9120\n"
    "v_mfma_f32_16x16x32_bf16 v[92:95], a[44:47], v[184:187], v[92:95]\n"
    "ds_read_b64_tr_b16 a[24:25], v2 offset:8640\n"
    "v_mbcnt_lo_u32_b32 v45, -1, 0\n"
    "v_and_b32 v45, 7, v45\n"
    "v_lshlrev_b32 v45, 3, v45\n"
    "v_add_u32 v45, 393216, v45\n"
    "v_mfma_f32_16x16x32_bf16 v[96:99], a[48:51], v[184:187], v[96:99]\n"
    "ds_read_b64_tr_b16 a[26:27], v2 offset:9152\n"
    "s_waitcnt lgkmcnt(14)\n"
    "ds_read_b64_tr_b16 a[28:29], v2 offset:8672\n"
    "global_load_dwordx2 v[46:47], v45, %[ws] sc1\n"
    "v_mfma_f32_16x16x32_bf16 v[100:103], a[52:55], v[184:187], v[100:103]\n"
    "s_waitcnt lgkmcnt(14)\n"
    "ds_read_b64_tr_b16 a[30:31], v2 offset:9184\n"
    "v_mfma_f32_16x16x32_bf16 v[104:107], a[56:59], v[184:187], v[104:107]\n"
    "s_waitcnt lgkmcnt(14)\n"
    "ds_read_b32 v7, v5\n"
    "v_mfma_f32_16x16x32_bf16 v[108:111], a[60:63], v[184:187], v[108:111]\n"
    "v_mfma_f32_16x16x32_bf16 v[112:115], a[32:35], v[188:191], v[112:115]\n"
    "v_mfma_f32_16x16x32_bf16 v[116:119], a[36:39], v[188:191], v[116:119]\n"
    "s_waitcnt lgkmcnt(0)\n"
    "v_cmp_gt_u32 vcc, s46, v7\n"
    "v_mfma_f32_16x16x32_bf16 v[120:123], a[40:43], v[188:191], v[120:123]\n"
    "s_cbranch_vccnz Lfs_24_%=\n"
    "Lfb_23_%=:\n"
    "v_mfma_f32_16x16x32_bf16 v[124:127], a[44:47], v[188:191], v[124:127]\n"
    "s_add_u32 m0, s40, 42240\n"
    "s_xor_b32 s77, s42, 0x2000\n"
    "buffer_load_dwordx4 v3, s[24:27], s42 offen nt lds\n"
    "v_mfma_f32_16x16x32_bf16 v[128:131], a[48:51], v[188:191], v[128:131]\n"
    "v_mfma_f32_16x16x32_bf16 v[132:135], a[52:55], v[188:191], v[132:135]\n"
    "v_mfma_f32_16x16x32_bf16 v[136:139], a[56:59], v[188:191], v[136:139]\n"
    "s_add_u32 m0, m0, 1056\n"
    "s_add_u32 s42, s42, 107520\n"
    "buffer_load_dwordx4 v3, s[24:27], s77 offen nt lds\n"
    "s_and_b32 s42, s42, 0x25cff\n"
    "v_mfma_f32_16x16x32_bf16 v[140:143], a[60:63], v[188:191], v[140:143]\n"
    "s_setprio 3\n"
    "v_mfma_f32_16x16x32_bf16 v[48:51], a[0:3], v[144:147], v[48:51]\n"
    "ds_read_b64_tr_b16 a[32:33], v2 offset:16896\n"
    "ds_read_b64_tr_b16 a[34:35], v2 offset:17408\n"
    "v_mfma_f32_16x16x32_bf16 v[52:55], a[4:7], v[144:147], v[52:55]\n"
    "ds_read_b64_tr_b16 a[36:37], v2 offset:16928\n"
    "v_mfma_f32_16x16x32_bf16 v[56:59], a[8:11], v[144:147], v[56:59]\n"
    "ds_read_b64_tr_b16 a[38:39], v2 offset:17440\n"
    "v_mfma_f32_16x16x32_bf16 v[60:63], a[12:15], v[144:147], v[60:63]\n"
    "ds_read_b64_tr_b16 a[40:41], v2 offset:16960\n"
    "v_mfma_f32_16x16x32_bf16 v[64:67], a[16:19], v[144:147], v[64:67]\n"
    "ds_read_b64_tr_b16 a[42:43], v2 offset:17472\n"
    "v_mfma_f32_16x16x32_bf16 v[68:71], a[20:23], v[144:147], v[68:71]\n"
    "ds_read_b64_tr_b16 a[44:45], v2 offset:16992\n"
    "v_mfma_f32_16x16x32_bf16 v[72:75], a[24:27], v[144:147], v[72:75]\n"
    "ds_read_b64_tr_b16 a[46:47], v2 offset:17504\n"
    "v_mfma_f32_16x16x32_bf16 v[76:79], a[28:31], v[144:147], v[76:79]\n"
    "ds_read_b64_tr_b16 a[48:49], v2 offset:17024\n"
    "ds_read_b64_tr_b16 a[50:51], v2 offset:17536\n"
    "v_mfma_f32_16x16x32_bf16 v[80:83], a[0:3], v[148:151], v[80:83]\n"
    "ds_read_b64_tr_b16 a[52:53], v2 offset:17056\n"
    "s_waitcnt vmcnt(12)\n"
    "s_waitcnt vmcnt(9)\n"
    "ds_add_u32 v4, v6\n"
    "s_add_u32 s46, s46, 1\n"
    "s_setprio 0\n"
    "v_mfma_f32_16x16x32_bf16 v[84:87], a[4:7], v[148:151], v[84:87]\n"
    "ds_read_b64_tr_b16 a[54:55], v2 offset:17568\n"
    "v_mfma_f32_16x16x32_bf16 v[88:91], a[8:11], v[148:151], v[88:91]\n"
    "ds_read_b64_tr_b16 a[56:57], v2 offset:17088\n"
    "v_mfma_f32_16x16x32_bf16 v[92:95], a[12:15], v[148:151], v[92:95]\n"
    "ds_read_b64_tr_b16 a[58:59], v2 offset:17600\n"
    "v_mfma_f32_16x16x32_bf16 v[96:99], a[16:19], v[148:151], v[96:99]\n"
    "s_waitcnt lgkmcnt(14)\n"
    "ds_read_b64_tr_b16 a[60:61], v2 offset:17120\n"
    "v_mfma_f32_16x16x32_bf16 v[100:103], a[20:23], v[148:151], v[100:103]\n"
    "s_waitcnt lgkmcnt(14)\n"
    "ds_read_b64_tr_b16 a[62:63], v2 offset:17632\n"
    "v_mfma_f32_16x16x32_bf16 v[104:107], a[24:27], v[148:151], v[104:107]\n"
    "s_waitcnt lgkmcnt(14)\n"
    "ds_read_b32 v7, v5\n"
    "v_mfma_f32_16x16x32_bf16 v[108:111], a[28:31], v[148:151], v[108:111]\n"
    "v_mfma_f32_16x16x32_bf16 v[112:115], a[0:3], v[152:155], v[112:115]\n"
    "v_mfma_f32_16x16x32_bf16 v[116:119], a[4:7], v[152:155], v[116:119]\n"
    "s_waitcnt lgkmcnt(0)\n"
    "v_cmp_gt_u32 vcc, s46, v7\n"
    "v_mfma_f32_16x16x32_bf16 v[120:123], a[8:11], v[152:155], v[120:123]\n"
    "s_cbranch_vccnz Lfs_26_%=\n"
    "Lfb_25_%=:\n"
    "v_mfma_f32_16x16x32_bf16 v[124:127], a[12:15], v[152:155], v[124:127]\n"
    "s_add_u32 m0, s40, 8448\n"
    "s_xor_b32 s77, s41, 0x2000\n"
    "buffer_load_dwordx4 v3, s[20:23], s41 offen nt lds\n"
    "v_mfma_f32_16x16x32_bf16 v[128:131], a[16:19], v[152:155], v[128:131]\n"
    "v_mfma_f32_16x16x32_bf16 v[132:135], a[20:23], v[152:155], v[132:135]\n"
    "v_mfma_f32_16x16x32_bf16 v[136:139], a[24:27], v[152:155], v[136:139]\n"
    "s_add_u32 m0, m0, 1056\n"
    "s_add_u32 s41, s41, 107520\n"
    "buffer_load_dwordx4 v3, s[20:23], s77 offen nt lds\n"
    "s_and_b32 s41, s41, 0x25cff\n"
    "v_mfma_f32_16x16x32_bf16 v[140:143], a[28:31], v[152:155], v[140:143]\n"
    "s_setprio 3\n"
    "v_mfma_f32_16x16x32_bf16 v[48:51], a[32:35], v[156:159], v[48:51]\n"
    "ds_read_b128 v[168:171], v32 offset:0\n"
    "ds_read_b128 v[172:175], v32 offset:2048\n"
    "v_mfma_f32_16x16x32_bf16 v[52:55], a[36:39], v[156:159], v[52:55]\n"
    "ds_read_b128 v[176:179], v32 offset:4096\n"
    "ds_read_b128 v[180:183], v33 offset:0\n"
    "v_mfma_f32_16x16x32_bf16 v[56:59], a[40:43], v[156:159], v[56:59]\n"
    "ds_read_b128 v[184:187], v33 offset:2048\n"
    "v_mfma_f32_16x16x32_bf16 v[60:63], a[44:47], v[156:159], v[60:63]\n"
    "ds_read_b128 v[188:191], v33 offset:4096\n"
    "ds_read_b64_tr_b16 a[0:1], v2 offset:25344\n"
    "v_mfma_f32_16x16x32_bf16 v[64:67], a[48:51], v[156:159], v[64:67]\n"
    "ds_read_b64_tr_b16 a[2:3], v2 offset:25856\n"
    "v_mfma_f32_16x16x32_bf16 v[68:71], a[52:55], v[156:159], v[68:71]\n"
    "ds_read_b64_tr_b16 a[4:5], v2 offset:25376\n"
    "ds_read_b64_tr_b16 a[6:7], v2 offset:25888\n"
    "v_mfma_f32_16x16x32_bf16 v[72:75], a[56:59], v[156:159], v[72:75]\n"
    "ds_read_b64_tr_b16 a[8:9], v2 offset:25408\n"
    "v_mfma_f32_16x16x32_bf16 v[76:79], a[60:63], v[156:159], v[76:79]\n"
    "ds_read_b64_tr_b16 a[10:11], v2 offset:25920\n"
    "ds_read_b64_tr_b16 a[12:13], v2 offset:25440\n"
    "v_mfma_f32_16x16x32_bf16 v[80:83], a[32:35], v[160:163], v[80:83]\n"
    "ds_read_b64_tr_b16 a[14:15], v2 offset:25952\n"
    "ds_read_b64_tr_b16 a[16:17], v2 offset:25472\n"
    "s_waitcnt lgkmcnt(9)\n"
    "s_waitcnt vmcnt(9)\n"
    "ds_add_u32 v4, v6\n"
    "s_add_u32 s46, s46, 1\n"
    "s_setprio 0\n"
    "v_mfma_f32_16x16x32_bf16 v[84:87], a[36:39], v[160:163], v[84:87]\n"
    "ds_read_b64_tr_b16 a[18:19], v2 offset:25984\n"
    "v_mfma_f32_16x16x32_bf16 v[88:91], a[40:43], v[160:163], v[88:91]\n"
    "ds_read_b64_tr_b16 a[20:21], v2 offset:25504\n"
    "ds_read_b64_tr_b16 a[22:23], v2 offset:26016\n"
    "v_mfma_f32_16x16x32_bf16 v[92:95], a[44:47], v[160:163], v[92:95]\n"
    "ds_read_b64_tr_b16 a[24:25], v2 offset:25536\n"
    "v_mfma_f32_16x16x32_bf16 v[96:99], a[48:51], v[160:163], v[96:99]\n"
    "ds_read_b64_tr_b16 a[26:27], v2 offset:26048\n"
    "s_waitcnt lgkmcnt(14)\n"
    "ds_read_b64_tr_b16 a[28:29], v2 offset:25568\n"
    "v_mfma_f32_16x16x32_bf16 v[100:103], a[52:55], v[160:163], v[100:103]\n"
    "s_waitcnt lgkmcnt(14)\n"
    "ds_read_b64_tr_b16 a[30:31], v2 offset:26080\n"
    "v_mfma_f32_16x16x32_bf16 v[104:107], a[56:59], v[160:163], v[104:107]\n"
    "s_waitcnt lgkmcnt(14)\n"
    "ds_read_b32 v7, v5\n"
    "v_mfma_f32_16x16x32_bf16 v[108:111], a[60:63], v[160:163], v[108:111]\n"
    "v_mfma_f32_16x16x32_bf16 v[112:115], a[32:35], v[164:167], v[112:115]\n"
    "v_mfma_f32_16x16x32_bf16 v[116:119], a[36:39], v[164:167], v[116:119]\n"
    "s_waitcnt lgkmcnt(0)\n"
    "v_cmp_gt_u32 vcc, s46, v7\n"
    "v_mfma_f32_16x16x32_bf16 v[120:123], a[40:43], v[164:167], v[120:123]\n"
    "s_cbranch_vccnz Lfs_28_%=\n"
    "Lfb_27_%=:\n"
    "v_mov_b32 v5, v34\n"
    "s_setprio 1\n"
    "s_cmp_lg_u32 %[team], 0\n"
    "s_cbranch_scc1 Lp0_%=\n"
    "s_setprio 2\n"
    "Lp0_%=:\n"
    "v_mfma_f32_16x16x32_bf16 v[124:127], a[44:47], v[164:167], v[124:127]\n"
    "s_add_u32 m0, s40, 16896\n"
    "s_xor_b32 s77, s41, 0x2000\n"
    "buffer_load_dwordx4 v3, s[20:23], s41 offen nt lds\n"
    "v_mfma_f32_16x16x32_bf16 v[128:131], a[48:51], v[164:167], v[128:131]\n"
    "v_mfma_f32_16x16x32_bf16 v[132:135], a[52:55], v[164:167], v[132:135]\n"
    "v_mfma_f32_16x16x32_bf16 v[136:139], a[56:59], v[164:167], v[136:139]\n"
    "s_add_u32 m0, m0, 1056\n"
    "s_add_u32 s41, s41, 107520\n"
    "buffer_load_dwordx4 v3, s[20:23], s77 offen nt lds\n"
    "s_and_b32 s41, s41, 0x25cff\n"
    "v_mfma_f32_16x16x32_bf16 v[140:143], a[60:63], v[164:167], v[140:143]\n"
    "v_mfma_f32_16x16x32_bf16 v[48:51], a[0:3], v[168:171], v[48:51]\n"
    "ds_read_b64_tr_b16 a[32:33], v2 offset:33792\n"
    "ds_read_b64_tr_b16 a[34:35], v2 offset:34304\n"
    "v_mfma_f32_16x16x32_bf16 v[52:55], a[4:7], v[168:171], v[52:55]\n"
    "s_add_u32 m0, s40, 50688\n"
    "s_xor_b32 s77, s42, 0x2000\n"
    "buffer_load_dwordx4 v3, s[24:27], s42 offen nt lds\n"
    "ds_read_b64_tr_b16 a[36:37], v2 offset:33824\n"
    "v_mfma_f32_16x16x32_bf16 v[56:59], a[8:11], v[168:171], v[56:59]\n"
    "ds_read_b64_tr_b16 a[38:39], v2 offset:34336\n"
    "v_mfma_f32_16x16x32_bf16 v[60:63], a[12:15], v[168:171], v[60:63]\n"
    "ds_read_b64_tr_b16 a[40:41], v2 offset:33856\n"
    "v_mfma_f32_16x16x32_bf16 v[64:67], a[16:19], v[168:171], v[64:67]\n"
    "ds_read_b64_tr_b16 a[42:43], v2 offset:34368\n"
    "v_mfma_f32_16x16x32_bf16 v[68:71], a[20:23], v[168:171], v[68:71]\n"
    "s_add_u32 m0, m0, 1056\n"
    "s_add_u32 s42, s42, 107520\n"
    "buffer_load_dwordx4 v3, s[24:27], s77 offen nt lds\n"
    "s_and_b32 s42, s42, 0x25cff\n"
    "ds_read_b64_tr_b16 a[44:45], v2 offset:33888\n"
    "v_mfma_f32_16x16x32_bf16 v[72:75], a[24:27], v[168:171], v[72:75]\n"
    "ds_read_b64_tr_b16 a[46:47], v2 offset:34400\n"
    "v_mfma_f32_16x16x32_bf16 v[76:79], a[28:31], v[168:171], v[76:79]\n"
    "ds_read_b64_tr_b16 a[48:49], v2 offset:33920\n"
    "ds_read_b64_tr_b16 a[50:51], v2 offset:34432\n"
    "v_mfma_f32_16x16x32_bf16 v[80:83], a[0:3], v[172:175], v[80:83]\n"
    "ds_read_b64_tr_b16 a[52:53], v2 offset:33952\n"
    "s_waitcnt vmcnt(9)\n"
    "ds_add_u32 v4, v6\n"
    "s_add_u32 s46, s46, 1\n"
    "v_mfma_f32_16x16x32_bf16 v[84:87], a[4:7], v[172:175], v[84:87]\n"
    "ds_read_b64_tr_b16 a[54:55], v2 offset:34464\n"
    "v_mfma_f32_16x16x32_bf16 v[88:91], a[8:11], v[172:175], v[88:91]\n"
    "ds_read_b64_tr_b16 a[56:57], v2 offset:33984\n"
    "v_mfma_f32_16x16x32_bf16 v[92:95], a[12:15], v[172:175], v[92:95]\n"
    "ds_read_b64_tr_b16 a[58:59], v2 offset:34496\n"
    "v_mfma_f32_16x16x32_bf16 v[96:99], a[16:19], v[172:175], v[96:99]\n"
    "s_waitcnt lgkmcnt(14)\n"
    "ds_read_b64_tr_b16 a[60:61], v2 offset:34016\n"
    "v_mfma_f32_16x16x32_bf16 v[100:103], a[20:23], v[172:175], v[100:103]\n"
    "s_waitcnt lgkmcnt(14)\n"
    "ds_read_b64_tr_b16 a[62:63], v2 offset:34528\n"
    "v_mfma_f32_16x16x32_bf16 v[104:107], a[24:27], v[172:175], v[104:107]\n"
    "s_waitcnt lgkmcnt(14)\n"
    "ds_read_b32 v7, v5\n"
    "v_mfma_f32_16x16x32_bf16 v[108:111], a[28:31], v[172:175], v[108:111]\n"
    "v_mfma_f32_16x16x32_bf16 v[112:115], a[0:3], v[176:179], v[112:115]\n"
    "v_mfma_f32_16x16x32_bf16 v[116:119], a[4:7], v[176:179], v[116:119]\n"
    "s_waitcnt lgkmcnt(0)\n"
    "v_cmp_gt_u32 vcc, s46, v7\n"
    "v_mfma_f32_16x16x32_bf16 v[120:123], a[8:11], v[176:179], v[120:123]\n"
    "s_cbranch_vccnz Lfs_30_%=\n"
    "Lfb_29_%=:\n"
    "v_mfma_f32_16x16x32_bf16 v[124:127], a[12:15], v[176:179], v[124:127]\n"
    "s_add_u32 m0, s40, 25344\n"
    "s_xor_b32 s77, s41, 0x2000\n"
    "buffer_load_dwordx4 v3, s[20:23], s41 offen nt lds\n"
    "v_mfma_f32_16x16x32_bf16 v[128:131], a[16:19], v[176:179], v[128:131]\n"
    "v_mfma_f32_16x16x32_bf16 v[132:135], a[20:23], v[176:179], v[132:135]\n"
    "v_mfma_f32_16x16x32_bf16 v[136:139], a[24:27], v[176:179], v[136:139]\n"
    "s_add_u32 m0, m0, 1056\n"
    "s_add_u32 s41, s41, 107520\n"
    "buffer_load_dwordx4 v3, s[20:23], s77 offen nt lds\n"
    "s_and_b32 s41, s41, 0x25cff\n"
    "v_mfma_f32_16x16x32_bf16 v[140:143], a[28:31], v[176:179], v[140:143]\n"
    "v_mfma_f32_16x16x32_bf16 v[48:51], a[32:35], v[180:183], v[48:51]\n"
    "ds_read_b128 a[0:3], v1 offset:0\n"
    "v_mfma_f32_16x16x32_bf16 v[52:55], a[36:39], v[180:183], v[52:55]\n"
    "s_add_u32 m0, s40, 59136\n"
    "s_xor_b32 s77, s42, 0x2000\n"
    "buffer_load_dwordx4 v3, s[24:27], s42 offen nt lds\n"
    "ds_read_b128 a[4:7], v1 offset:64\n"
    "v_mfma_f32_16x16x32_bf16 v[56:59], a[40:43], v[180:183], v[56:59]\n"
    "s_waitcnt vmcnt(11)\n"
    "v_mfma_f32_16x16x32_bf16 v[60:63], a[44:47], v[180:183], v[60:63]\n"
    "ds_read_b128 a[8:11], v1 offset:128\n"
    "v_subrev_u32 v46, s80, v46\n"
    "v_add_u32 v45, 400, v46\n"
    "v_mfma_f32_16x16x32_bf16 v[64:67], a[48:51], v[180:183], v[64:67]\n"
    "v_cmp_gt_u32 vcc, 800, v45\n"
    "v_mov_b32 v45, s72\n"
    "s_nop 0\n"
    "v_cndmask_b32 v46, 0, v46, vcc\n"
    "v_cndmask_b32 v47, v45, v47, vcc\n"
    "v_mfma_f32_16x16x32_bf16 v[68:71], a[52:55], v[180:183], v[68:71]\n"
    "s_add_u32 m0, m0, 1056\n"
    "s_add_u32 s42, s42, 107520\n"
    "buffer_load_dwordx4 v3, s[24:27], s77 offen nt lds\n"
    "s_and_b32 s42, s42, 0x25cff\n"
    "ds_read_b128 a[12:15], v1 offset:192\n"
    "s_nop 1\n"
    "v_add_u32_dpp v46, v46, v46 row_shr:1 row_mask:0xf bank_mask:0xf bound_ctrl:0\n"
    "v_min_u32_dpp v47, v47, v47 row_shr:1 row_mask:0xf bank_mask:0xf\n"
    "v_mfma_f32_16x16x32_bf16 v[72:75], a[56:59], v[180:183], v[72:75]\n"
    "s_nop 1\n"
    "v_add_u32_dpp v46, v46, v46 row_shr:2 row_mask:0xf bank_mask:0xf bound_ctrl:0\n"
    "v_min_u32_dpp v47, v47, v47 row_shr:2 row_mask:0xf bank_mask:0xf\n"
    "v_mfma_f32_16x16x32_bf16 v[76:79], a[60:63], v[180:183], v[76:79]\n"
    "ds_read_b128 a[28:31], v1 offset:704\n"
    "s_nop 1\n"
    "v_add_u32_dpp v46, v46, v46 row_shr:4 row_mask:0xf bank_mask:0xf bound_ctrl:0\n"
    "v_min_u32_dpp v47, v47, v47 row_shr:4 row_mask:0xf bank_mask:0xf\n"
    "v_mfma_f32_16x16x32_bf16 v[80:83], a[32:35], v[184:187], v[80:83]\n"
    "ds_read_b128 a[24:27], v1 offset:640\n"
    "v_rcp_f32 v47, v47\n"
    "v_mfma_f32_16x16x32_bf16 v[84:87], a[36:39], v[184:187], v[84:87]\n"
    "s_nop 0\n"
    "v_mul_f32 v47, s72, v47\n"
    "v_add_f32 v47, -1.0, v47\n"
    "v_mul_f32 v47, 0x44c80000, v47\n"
    "v_cvt_i32_f32 v47, v47\n"
    "v_mfma_f32_16x16x32_bf16 v[88:91], a[40:43], v[184:187], v[88:91]\n"
    "ds_read_b128 a[20:23], v1 offset:576\n"
    "s_nop 1\n"
    "v_readlane_b32 s54, v46, 7\n"
    "v_readlane_b32 s55, v47, 7\n"
    "s_nop 1\n"
    "s_ashr_i32 s54, s54, 3\n"
    "s_add_u32 s82, s82, s54\n"
    "s_min_i32 s55, s55, 110\n"
    "s_max_i32 s55, s55, 0\n"
    "s_add_u32 s82, s82, s55\n"
    "v_mfma_f32_16x16x32_bf16 v[92:95], a[44:47], v[184:187], v[92:95]\n"
    "v_mfma_f32_16x16x32_bf16 v[96:99], a[48:51], v[184:187], v[96:99]\n"
    "ds_read_b128 a[16:19], v1 offset:512\n"
    "v_mfma_f32_16x16x32_bf16 v[100:103], a[52:55], v[184:187], v[100:103]\n"
    "v_mfma_f32_16x16x32_bf16 v[104:107], a[56:59], v[184:187], v[104:107]\n"
    "v_mfma_f32_16x16x32_bf16 v[108:111], a[60:63], v[184:187], v[108:111]\n"
    "v_mfma_f32_16x16x32_bf16 v[112:115], a[32:35], v[188:191], v[112:115]\n"
    "s_lshl_b32 s54, %[team], 2\n"
    "s_add_u32 s54, s54, %[w]\n"
    "s_lshl_b32 s54, s54, 8\n"
    "s_add_u32 s54, s54, %[lds]\n"
    "s_add_u32 s54, s54, 156160\n"
    "v_and_b32 v43, 0x380f0, v3\n"
    "v_min_u32 v43, 0xf0, v43\n"
    "v_add_u32 v43, s54, v43\n"
    "v_sub_u32 v42, 0, v39\n"
    "v_and_b32 v42, v42, v39\n"
    "v_add_u32 v38, v40, v42\n"
    "v_and_b32 v38, v38, v39\n"
    "v_bfi_b32 v41, v39, v38, v41\n"
    "s_mul_i32 s54, %[team], 12\n"
    "s_sub_u32 exec_lo, 0x7f, s54\n"
    "s_mov_b32 exec_hi, 0\n"
    "global_store_dword v37, v41, %[ws] sc1\n"
    "s_mov_b64 exec, 0xffff\n"
    "ds_write_b128 v43, v[36:39]\n"
    "s_mov_b64 exec, -1\n"
    "v_mfma_f32_16x16x32_bf16 v[116:119], a[36:39], v[188:191], v[116:119]\n"
    "v_mfma_f32_16x16x32_bf16 v[120:123], a[40:43], v[188:191], v[120:123]\n"
    "v_mfma_f32_16x16x32_bf16 v[124:127], a[44:47], v[188:191], v[124:127]\n"
    "v_mfma_f32_16x16x32_bf16 v[128:131], a[48:51], v[188:191], v[128:131]\n"
    "v_mfma_f32_16x16x32_bf16 v[132:135], a[52:55], v[188:191], v[132:135]\n"
    "v_mfma_f32_16x16x32_bf16 v[136:139], a[56:59], v[188:191], v[136:139]\n"
    "v_mfma_f32_16x16x32_bf16 v[140:143], a[60:63], v[188:191], v[140:143]\n"
    "s_nop 7\n"
    "v_mul_f32 v48, s47, v48\n"
    "v_mul_f32 v49, s47, v49\n"
    "v_mul_f32 v50, s47, v50\n"
    "v_mul_f32 v51, s47, v51\n"
    "v_mul_f32 v52, s47, v52\n"
    "v_mul_f32 v53, s47, v53\n"
    "v_mul_f32 v54, s47, v54\n"
    "v_mul_f32 v55, s47, v55\n"
    "v_cvt_pk_bf16_f32 v144, v48, v49\n"
    "v_cvt_pk_bf16_f32 v145, v50, v51\n"
    "v_cvt_pk_bf16_f32 v146, v52, v53\n"
    "v_cvt_pk_bf16_f32 v147, v54, v55\n"
    "s_nop 1\n"
    "v_permlane32_swap_b32 v144, v146\n"
    "v_permlane32_swap_b32 v145, v147\n"
    "v_mul_f32 v56, s47, v56\n"
    "v_mul_f32 v57, s47, v57\n"
    "v_mul_f32 v58, s47, v58\n"
    "v_mul_f32 v59, s47, v59\n"
    "v_mul_f32 v60, s47, v60\n"
    "v_mul_f32 v61, s47, v61\n"
    "v_mul_f32 v62, s47, v62\n"
    "v_mul_f32 v63, s47, v63\n"
    "v_cvt_pk_bf16_f32 v148, v56, v57\n"
    "v_cvt_pk_bf16_f32 v149, v58, v59\n"
    "v_cvt_pk_bf16_f32 v150, v60, v61\n"
    "v_cvt_pk_bf16_f32 v151, v62, v63\n"
    "v_permlane16_swap_b32 v144, v146\n"
    "v_permlane16_swap_b32 v145, v147\n"
    "v_permlane32_swap_b32 v148, v150\n"
    "v_permlane32_swap_b32 v149, v151\n"
    "v_mul_f32 v64, s47, v64\n"
    "v_mul_f32 v65, s47, v65\n"
    "v_mul_f32 v66, s47, v66\n"
    "v_mul_f32 v67, s47, v67\n"
    "v_mul_f32 v68, s47, v68\n"
    "v_mul_f32 v69, s47, v69\n"
    "v_mul_f32 v70, s47, v70\n"
    "v_mul_f32 v71, s47, v71\n"
    "v_cvt_pk_bf16_f32 v152, v64, v65\n"
    "v_cvt_pk_bf16_f32 v153, v66, v67\n"
    "v_cvt_pk_bf16_f32 v154, v68, v69\n"
    "v_cvt_pk_bf16_f32 v155, v70, v71\n"
    "v_permlane16_swap_b32 v148, v150\n"
    "v_permlane16_swap_b32 v149, v151\n"
    "v_permlane32_swap_b32 v152, v154\n"
    "v_permlane32_swap_b32 v153, v155\n"
    "v_mul_f32 v72, s47, v72\n"
    "v_mul_f32 v73, s47, v73\n"
    "v_mul_f32 v74, s47, v74\n"
    "v_mul_f32 v75, s47, v75\n"
    "v_mul_f32 v76, s47, v76\n"
    "v_mul_f32 v77, s47, v77\n"
    "v_mul_f32 v78, s47, v78\n"
    "v_mul_f32 v79, s47, v79\n"
    "v_cvt_pk_bf16_f32 v156, v72, v73\n"
    "v_cvt_pk_bf16_f32 v157, v74, v75\n"
    "v_cvt_pk_bf16_f32 v158, v76, v77\n"
    "v_cvt_pk_bf16_f32 v159, v78, v79\n"
    "v_permlane16_swap_b32 v152, v154\n"
    "v_permlane16_swap_b32 v153, v155\n"
    "v_permlane32_swap_b32 v156, v158\n"
    "v_permlane32_swap_b32 v157, v159\n"
    "v_mul_f32 v80, s47, v80\n"
    "v_mul_f32 v81, s47, v81\n"
    "v_mul_f32 v82, s47, v82\n"
    "v_mul_f32 v83, s47, v83\n"
    "v_mul_f32 v84, s47, v84\n"
    "v_mul_f32 v85, s47, v85\n"
    "v_mul_f32 v86, s47, v86\n"
    "v_mul_f32 v87, s47, v87\n"
    "v_cvt_pk_bf16_f32 v160, v80, v81\n"
    "v_cvt_pk_bf16_f32 v161, v82, v83\n"
    "v_cvt_pk_bf16_f32 v162, v84, v85\n"
    "v_cvt_pk_bf16_f32 v163, v86, v87\n"
    "v_permlane16_swap_b32 v156, v158\n"
    "v_permlane16_swap_b32 v157, v159\n"
    "v_permlane32_swap_b32 v160, v162\n"
    "v_permlane32_swap_b32 v161, v163\n"
    "v_mul_f32 v88, s47, v88\n"
    "v_mul_f32 v89, s47, v89\n"
    "v_mul_f32 v90, s47, v90\n"
    "v_mul_f32 v91, s47, v91\n"
    "v_mul_f32 v92, s47, v92\n"
    "v_mul_f32 v93, s47, v93\n"
    "v_mul_f32 v94, s47, v94\n"
    "v_mul_f32 v95, s47, v95\n"
    "v_cvt_pk_bf16_f32 v164, v88, v89\n"
    "v_cvt_pk_bf16_f32 v165, v90, v91\n"
    "v_cvt_pk_bf16_f32 v166, v92, v93\n"
    "v_cvt_pk_bf16_f32 v167, v94, v95\n"
    "v_permlane16_swap_b32 v160, v162\n"
    "v_permlane16_swap_b32 v161, v163\n"
    "v_permlane32_swap_b32 v164, v166\n"
    "v_permlane32_swap_b32 v165, v167\n"
    "v_mul_f32 v96, s47, v96\n"
    "v_mul_f32 v97, s47, v97\n"
    "v_mul_f32 v98, s47, v98\n"
    "v_mul_f32 v99, s47, v99\n"
    "v_mul_f32 v100, s47, v100\n"
    "v_mul_f32 v101, s47, v101\n"
    "v_mul_f32 v102, s47, v102\n"
    "v_mul_f32 v103, s47, v103\n"
    "v_cvt_pk_bf16_f32 v168, v96, v97\n"
    "v_cvt_pk_bf16_f32 v169, v98, v99\n"
    "v_cvt_pk_bf16_f32 v170, v100, v101\n"
    "v_cvt_pk_bf16_f32 v171, v102, v103\n"
    "v_permlane16_swap_b32 v164, v166\n"
    "v_permlane16_swap_b32 v165, v167\n"
    "v_permlane32_swap_b32 v168, v170\n"
    "v_permlane32_swap_b32 v169, v171\n"
    "s_waitcnt vmcnt(9)\n"
    "ds_add_u32 v4, v6\n"
    "s_add_u32 s46, s46, 1\n"
    "v_mul_f32 v104, s47, v104\n"
    "v_mul_f32 v105, s47, v105\n"
    "v_mul_f32 v106, s47, v106\n"
    "v_mul_f32 v107, s47, v107\n"
    "v_mul_f32 v108, s47, v108\n"
    "v_mul_f32 v109, s47, v109\n"
    "v_mul_f32 v110, s47, v110\n"
    "v_mul_f32 v111, s47, v111\n"
    "v_cvt_pk_bf16_f32 v172, v104, v105\n"
    "v_cvt_pk_bf16_f32 v173, v106, v107\n"
    "v_cvt_pk_bf16_f32 v174, v108, v109\n"
    "v_cvt_pk_bf16_f32 v175, v110, v111\n"
    "v_permlane16_swap_b32 v168, v170\n"
    "v_permlane16_swap_b32 v169, v171\n"
    "v_permlane32_swap_b32 v172, v174\n"
    "v_permlane32_swap_b32 v173, v175\n"
    "v_mul_f32 v112, s47, v112\n"
    "v_mul_f32 v113, s47, v113\n"
    "v_mul_f32 v114, s47, v114\n"
    "v_mul_f32 v115, s47, v115\n"
    "v_mul_f32 v116, s47, v116\n"
    "v_mul_f32 v117, s47, v117\n"
    "v_mul_f32 v118, s47, v118\n"
    "v_mul_f32 v119, s47, v119\n"
    "v_cvt_pk_bf16_f32 v176, v112, v113\n"
    "v_cvt_pk_bf16_f32 v177, v114, v115\n"
    "v_cvt_pk_bf16_f32 v178, v116, v117\n"
    "v_cvt_pk_bf16_f32 v179, v118, v119\n"
    "v_permlane16_swap_b32 v172, v174\n"
    "v_permlane16_swap_b32 v173, v175\n"
    "v_permlane32_swap_b32 v176, v178\n"
    "v_permlane32_swap_b32 v177, v179\n"
    "v_mul_f32 v120, s47, v120\n"
    "v_mul_f32 v121, s47, v121\n"
    "v_mul_f32 v122, s47, v122\n"
    "v_mul_f32 v123, s47, v123\n"
    "v_mul_f32 v124, s47, v124\n"
    "v_mul_f32 v125, s47, v125\n"
    "v_mul_f32 v126, s47, v126\n"
    "v_mul_f32 v127, s47, v127\n"
    "v_cvt_pk_bf16_f32 v180, v120, v121\n"
    "v_cvt_pk_bf16_f32 v181, v122, v123\n"
    "v_cvt_pk_bf16_f32 v182, v124, v125\n"
    "v_cvt_pk_bf16_f32 v183, v126, v127\n"
    "v_permlane16_swap_b32 v176, v178\n"
    "v_permlane16_swap_b32 v177, v179\n"
    "v_permlane32_swap_b32 v180, v182\n"
    "v_permlane32_swap_b32 v181, v183\n"
    "v_mul_f32 v128, s47, v128\n"
    "v_mul_f32 v129, s47, v129\n"
    "v_mul_f32 v130, s47, v130\n"
    "v_mul_f32 v131, s47, v131\n"
    "v_mul_f32 v132, s47, v132\n"
    "v_mul_f32 v133, s47, v133\n"
    "v_mul_f32 v134, s47, v134\n"
    "v_mul_f32 v135, s47, v135\n"
    "v_cvt_pk_bf16_f32 v184, v128, v129\n"
    "v_cvt_pk_bf16_f32 v185, v130, v131\n"
    "v_cvt_pk_bf16_f32 v186, v132, v133\n"
    "v_cvt_pk_bf16_f32 v187, v134, v135\n"
    "v_permlane16_swap_b32 v180, v182\n"
    "v_permlane16_swap_b32 v181, v183\n"
    "v_permlane32_swap_b32 v184, v186\n"
    "v_permlane32_swap_b32 v185, v187\n"
    "ds_read_b32 v7, v5\n"
    "v_mul_f32 v136, s47, v136\n"
    "v_mul_f32 v137, s47, v137\n"
    "v_mul_f32 v138, s47, v138\n"
    "v_mul_f32 v139, s47, v139\n"
    "v_mul_f32 v140, s47, v140\n"
    "v_mul_f32 v141, s47, v141\n"
    "v_mul_f32 v142, s47, v142\n"
    "v_mul_f32 v143, s47, v143\n"
    "v_cvt_pk_bf16_f32 v188, v136, v137\n"
    "v_cvt_pk_bf16_f32 v189, v138, v139\n"
    "v_cvt_pk_bf16_f32 v190, v140, v141\n"
    "v_cvt_pk_bf16_f32 v191, v142, v143\n"
    "v_permlane16_swap_b32 v184, v186\n"
    "v_permlane16_swap_b32 v185, v187\n"
    "v_permlane32_swap_b32 v188, v190\n"
    "v_permlane32_swap_b32 v189, v191\n"
    "s_nop 1\n"
    "v_permlane16_swap_b32 v188, v190\n"
    "v_permlane16_swap_b32 v189, v191\n"
    "s_waitcnt lgkmcnt(0)\n"
    "v_cmp_gt_u32 vcc, s46, v7\n"
    "s_cbranch_vccnz Lfs_32_%=\n"
    "Lfb_31_%=:\n"
    "v_add_u32 v2, 16896, v2\n"
    "s_memrealtime s[80:81]\n"
    "v_mfma_f32_16x16x32_bf16 v[24:27], a[0:3], v[144:147], 0\n"
    "v_mfma_f32_16x16x32_bf16 v[24:27], a[4:7], v[148:151], v[24:27]\n"
    "v_mfma_f32_16x16x32_bf16 v[24:27], a[8:11], v[152:155], v[24:27]\n"
    "v_mfma_f32_16x16x32_bf16 v[24:27], a[12:15], v[156:159], v[24:27]\n"
    "v_mfma_f32_16x16x32_bf16 v[28:31], a[28:31], v[156:159], 0\n"
    "v_mfma_f32_16x16x32_bf16 v[28:31], a[24:27], v[152:155], v[28:31]\n"
    "v_mfma_f32_16x16x32_bf16 v[28:31], a[20:23], v[148:151], v[28:31]\n"
    "v_mfma_f32_16x16x32_bf16 v[28:31], a[16:19], v[144:147], v[28:31]\n"
    "v_mfma_f32_16x16x32_bf16 v[32:35], a[0:3], v[160:163], 0\n"
    "v_mfma_f32_16x16x32_bf16 v[32:35], a[4:7], v[164:167], v[32:35]\n"
    "v_mfma_f32_16x16x32_bf16 v[32:35], a[8:11], v[168:171], v[32:35]\n"
    "v_mfma_f32_16x16x32_bf16 v[32:35], a[12:15], v[172:175], v[32:35]\n"
    "v_mfma_f32_16x16x32_bf16 v[36:39], a[28:31], v[172:175], 0\n"
    "v_mfma_f32_16x16x32_bf16 v[36:39], a[24:27], v[168:171], v[36:39]\n"
    "v_mfma_f32_16x16x32_bf16 v[36:39], a[20:23], v[164:167], v[36:39]\n"
    "v_mfma_f32_16x16x32_bf16 v[36:39], a[16:19], v[160:163], v[36:39]\n"
    "v_mfma_f32_16x16x32_bf16 v[40:43], a[0:3], v[176:179], 0\n"
    "v_mfma_f32_16x16x32_bf16 v[40:43], a[4:7], v[180:183], v[40:43]\n"
    "v_mfma_f32_16x16x32_bf16 v[40:43], a[8:11], v[184:187], v[40:43]\n"
    "v_mfma_f32_16x16x32_bf16 v[40:43], a[12:15], v[188:191], v[40:43]\n"
    "v_mfma_f32_16x16x32_bf16 v[44:47], a[28:31], v[188:191], 0\n"
    "v_mfma_f32_16x16x32_bf16 v[44:47], a[24:27], v[184:187], v[44:47]\n"
    "v_mfma_f32_16x16x32_bf16 v[44:47], a[20:23], v[180:183], v[44:47]\n"
    "v_mfma_f32_16x16x32_bf16 v[44:47], a[16:19], v[176:179], v[44:47]\n"
    "s_nop 7\n"
    "v_max3_f32 v8, v24, v25, v26\n"
    "v_max3_f32 v8, v8, v27, v28\n"
    "v_max3_f32 v8, v8, v29, v30\n"
    "v_max_f32 v8, v8, v31\n"
    "v_max3_f32 v9, v32, v33, v34\n"
    "v_max3_f32 v9, v9, v35, v36\n"
    "v_max3_f32 v9, v9, v37, v38\n"
    "v_max_f32 v9, v9, v39\n"
    "v_max3_f32 v10, v40, v41, v42\n"
    "v_max3_f32 v10, v10, v43, v44\n"
    "v_max3_f32 v10, v10, v45, v46\n"
    "v_max_f32 v10, v10, v47\n"
    "s_waitcnt lgkmcnt(0)\n"
    "s_sub_u32 s54, s80, s82\n"
    "s_min_i32 s54, s54, 500\n"
    "s_max_i32 s54, s54, -500\n"
    "s_mul_i32 s54, s54, %[kan]\n"
    "s_add_u32 s54, s54, 128\n"
    "s_ashr_i32 s54, s54, 8\n"
    "s_sub_u32 s83, s83, s54\n"
    "Lnegm_%=:\n"
    "v_mov_b32 v11, v8\n"
    "s_nop 1\n"
    "v_permlane32_swap_b32 v8, v11\n"
    "s_nop 1\n"
    "v_max_f32 v8, v8, v11\n"
    "v_mov_b32 v11, v8\n"
    "s_nop 1\n"
    "v_permlane16_swap_b32 v8, v11\n"
    "s_nop 1\n"
    "v_max_f32 v8, v8, v11\n"
    "v_sub_f32 v12, 0, v8\n"
    "v_sub_f32 v13, 0, v8\n"
    "v_sub_f32 v14, 0, v8\n"
    "v_sub_f32 v15, 0, v8\n"
    "v_mov_b32 v11, v9\n"
    "s_nop 1\n"
    "v_permlane32_swap_b32 v9, v11\n"
    "s_nop 1\n"
    "v_max_f32 v9, v9, v11\n"
    "v_mov_b32 v11, v9\n"
    "s_nop 1\n"
    "v_permlane16_swap_b32 v9, v11\n"
    "s_nop 1\n"
    "v_max_f32 v9, v9, v11\n"
    "v_sub_f32 v16, 0, v9\n"
    "v_sub_f32 v17, 0, v9\n"
    "v_sub_f32 v18, 0, v9\n"
    "v_sub_f32 v19, 0, v9\n"
    "v_mov_b32 v11, v10\n"
    "s_nop 1\n"
    "v_permlane32_swap_b32 v10, v11\n"
    "s_nop 1\n"
    "v_max_f32 v10, v10, v11\n"
    "v_mov_b32 v11, v10\n"
    "s_nop 1\n"
    "v_permlane16_swap_b32 v10, v11\n"
    "s_nop 1\n"
    "v_max_f32 v10, v10, v11\n"
    "v_sub_f32 v20, 0, v10\n"
    "v_sub_f32 v21, 0, v10\n"
    "v_sub_f32 v22, 0, v10\n"
    "v_sub_f32 v23, 0, v10\n"
    "v_mov_b32 v8, 0\n"
    "v_mov_b32 v9, 0\n"
    "v_mov_b32 v10, 0\n"
    "s_mov_b32 s67, 33792\n"
    "s_mov_b32 s64, 0\n"
    "s_mov_b32 s65, 8448\n"
    "s_mov_b32 s45, 7\n"
    "s_nop 1\n"
    "s_memrealtime s[80:81]\n"
    "s_waitcnt lgkmcnt(0)\n"
    "s_add_u32 s82, s80, s83\n"
    "v_mfma_f32_16x16x32_bf16 v[24:27], a[0:3], v[144:147], v[12:15]\n"
    "v_mov_b32 v112, 0\n"
    "v_mov_b32 v113, 0\n"
    "v_mfma_f32_16x16x32_bf16 v[24:27], a[4:7], v[148:151], v[24:27]\n"
    "v_mov_b32 v114, 0\n"
    "v_mov_b32 v115, 0\n"
    "ds_read_b32 v7, v5\n"
    "v_mfma_f32_16x16x32_bf16 v[24:27], a[8:11], v[152:155], v[24:27]\n"
    "v_mov_b32 v116, 0\n"
    "v_mov_b32 v117, 0\n"
    "v_mfma_f32_16x16x32_bf16 v[24:27], a[12:15], v[156:159], v[24:27]\n"
    "v_mov_b32 v118, 0\n"
    "v_mov_b32 v119, 0\n"
    "v_mfma_f32_16x16x32_bf16 v[28:31], a[28:31], v[156:159], v[12:15]\n"
    "v_mov_b32 v120, 0\n"
    "v_mov_b32 v121, 0\n"
    "v_mfma_f32_16x16x32_bf16 v[28:31], a[24:27], v[152:155], v[28:31]\n"
    "v_mov_b32 v122, 0\n"
    "v_mov_b32 v123, 0\n"
    "v_mfma_f32_16x16x32_bf16 v[28:31], a[20:23], v[148:151], v[28:31]\n"
    "v_mov_b32 v124, 0\n"
    "v_mov_b32 v125, 0\n"
    "s_waitcnt lgkmcnt(0)\n"
    "v_cmp_gt_u32 vcc, s46, v7\n"
    "v_mfma_f32_16x16x32_bf16 v[28:31], a[16:19], v[144:147], v[28:31]\n"
    "v_mov_b32 v126, 0\n"
    "v_mov_b32 v127, 0\n"
    "v_exp_f32 v24, v24\n"
    "s_cbranch_vccnz Lfs_36_%=\n"
    "Lfb_35_%=:\n"
    "v_mov_b32 v128, 0\n"
    "v_mov_b32 v129, 0\n"
    "v_add_f32 v8, v8, v24\n"
    "s_add_u32 m0, s40, s67\n"
    "s_xor_b32 s77, s41, 0x2000\n"
    "buffer_load_dwordx4 v3, s[20:23], s41 offen nt lds\n"
    "v_mov_b32 v130, 0\n"
    "v_mov_b32 v131, 0\n"
    "v_exp_f32 v25, v25\n"
    "ds_read_b64_tr_b16 a[32:33], v2 offset:25344\n"
    "ds_read_b64_tr_b16 a[34:35], v2 offset:25856\n"
    "v_mov_b32 v132, 0\n"
    "v_mov_b32 v133, 0\n"
    "v_add_f32 v8, v8, v25\n"
    "v_cvt_pk_bf16_f32 v40, v24, v25\n"
    "ds_read_b64_tr_b16 a[36:37], v2 offset:25376\n"
    "ds_read_b64_tr_b16 a[38:39], v2 offset:25888\n"
    "v_mov_b32 v134, 0\n"
    "v_mov_b32 v135, 0\n"
    "v_exp_f32 v26, v26\n"
    "s_add_u32 m0, m0, 1056\n"
    "s_add_u32 s41, s41, 107520\n"
    "buffer_load_dwordx4 v3, s[20:23], s77 offen nt lds\n"
    "s_and_b32 s41, s41, 0x25cff\n"
    "ds_read_b64_tr_b16 a[40:41], v2 offset:25408\n"
    "ds_read_b64_tr_b16 a[42:43], v2 offset:25920\n"
    "v_mov_b32 v136, 0\n"
    "v_mov_b32 v137, 0\n"
    "v_add_f32 v8, v8, v26\n"
    "ds_read_b64_tr_b16 a[44:45], v2 offset:25440\n"
    "ds_read_b64_tr_b16 a[46:47], v2 offset:25952\n"
    "v_mov_b32 v138, 0\n"
    "v_mov_b32 v139, 0\n"
    "v_exp_f32 v27, v27\n"
    "ds_read_b64_tr_b16 a[48:49], v2 offset:25472\n"
    "ds_read_b64_tr_b16 a[50:51], v2 offset:25984\n"
    "v_mov_b32 v140, 0\n"
    "v_mov_b32 v141, 0\n"
    "v_add_f32 v8, v8, v27\n"
    "v_cvt_pk_bf16_f32 v41, v26, v27\n"
    "s_add_u32 m0, s40, 67584\n"
    "s_xor_b32 s77, s42, 0x2000\n"
    "buffer_load_dwordx4 v3, s[24:27], s42 offen nt lds\n"
    "ds_read_b64_tr_b16 a[52:53], v2 offset:25504\n"
    "ds_read_b64_tr_b16 a[54:55], v2 offset:26016\n"
    "v_mov_b32 v142, 0\n"
    "v_mov_b32 v143, 0\n"
    "v_exp_f32 v28, v28\n"
    "ds_read_b64_tr_b16 a[56:57], v2 offset:25536\n"
    "ds_read_b64_tr_b16 a[58:59], v2 offset:26048\n"
    "v_mfma_f32_16x16x32_bf16 v[32:35], a[0:3], v[160:163], v[16:19]\n"
    "v_add_f32 v8, v8, v28\n"
    "ds_read_b64_tr_b16 a[60:61], v2 offset:25568\n"
    "s_waitcnt lgkmcnt(14)\n"
    "ds_read_b64_tr_b16 a[62:63], v2 offset:26080\n"
    "v_mfma_f32_16x16x32_bf16 v[32:35], a[4:7], v[164:167], v[32:35]\n"
    "v_exp_f32 v29, v29\n"
    "s_add_u32 m0, m0, 1056\n"
    "s_add_u32 s42, s42, 107520\n"
    "buffer_load_dwordx4 v3, s[24:27], s77 offen nt lds\n"
    "s_and_b32 s42, s42, 0x25cff\n"
    "v_mfma_f32_16x16x32_bf16 v[32:35], a[8:11], v[168:171], v[32:35]\n"
    "v_add_f32 v8, v8, v29\n"
    "v_cvt_pk_bf16_f32 v42, v28, v29\n"
    "s_add_u32 s66, s65, 8448\n"
    "s_cmp_eq_u32 s66, 42240\n"
    "s_cselect_b32 s66, 0, s66\n"
    "v_mfma_f32_16x16x32_bf16 v[32:35], a[12:15], v[172:175], v[32:35]\n"
    "v_exp_f32 v30, v30\n"
    "v_mfma_f32_16x16x32_bf16 v[36:39], a[28:31], v[172:175], v[16:19]\n"
    "v_add_f32 v8, v8, v30\n"
    "v_mfma_f32_16x16x32_bf16 v[36:39], a[24:27], v[168:171], v[36:39]\n"
    "v_exp_f32 v31, v31\n"
    "v_mfma_f32_16x16x32_bf16 v[36:39], a[20:23], v[164:167], v[36:39]\n"
    "v_add_f32 v8, v8, v31\n"
    "v_cvt_pk_bf16_f32 v43, v30, v31\n"
    "v_mfma_f32_16x16x32_bf16 v[36:39], a[16:19], v[160:163], v[36:39]\n"
    "v_exp_f32 v32, v32\n"
    "s_waitcnt lgkmcnt(0)\n"
    "v_mfma_f32_16x16x32_bf16 v[48:51], a[32:35], v[40:43], 0\n"
    "v_add_f32 v9, v9, v32\n"
    "v_mfma_f32_16x16x32_bf16 v[52:55], a[36:39], v[40:43], 0\n"
    "v_exp_f32 v33, v33\n"
    "v_mfma_f32_16x16x32_bf16 v[56:59], a[40:43], v[40:43], 0\n"
    "v_add_f32 v9, v9, v33\n"
    "v_cvt_pk_bf16_f32 v44, v32, v33\n"
    "v_mfma_f32_16x16x32_bf16 v[60:63], a[44:47], v[40:43], 0\n"
    "v_exp_f32 v34, v34\n"
    "v_mfma_f32_16x16x32_bf16 v[64:67], a[48:51], v[40:43], 0\n"
    "v_add_f32 v9, v9, v34\n"
    "v_add_u32 v11, s65, v1\n"
    "v_mfma_f32_16x16x32_bf16 v[68:71], a[52:55], v[40:43], 0\n"
    "v_exp_f32 v35, v35\n"
    "v_mfma_f32_16x16x32_bf16 v[72:75], a[56:59], v[40:43], 0\n"
    "v_add_f32 v9, v9, v35\n"
    "v_cvt_pk_bf16_f32 v45, v34, v35\n"
    "v_mfma_f32_16x16x32_bf16 v[76:79], a[60:63], v[40:43], 0\n"
    "v_exp_f32 v36, v36\n"
    "s_waitcnt vmcnt(8)\n"
    "ds_add_u32 v4, v6\n"
    "s_add_u32 s46, s46, 1\n"
    "v_mfma_f32_16x16x32_bf16 v[24:27], a[0:3], v[176:179], v[20:23]\n"
    "v_add_f32 v9, v9, v36\n"
    "s_memrealtime s[80:81]\n"
    "v_mfma_f32_16x16x32_bf16 v[24:27], a[4:7], v[180:183], v[24:27]\n"
    "v_exp_f32 v37, v37\n"
    "ds_read_b128 a[0:3], v11 offset:0\n"
    "v_mfma_f32_16x16x32_bf16 v[24:27], a[8:11], v[184:187], v[24:27]\n"
    "v_add_f32 v9, v9, v37\n"
    "v_cvt_pk_bf16_f32 v46, v36, v37\n"
    "ds_read_b128 a[4:7], v11 offset:64\n"
    "v_mfma_f32_16x16x32_bf16 v[24:27], a[12:15], v[188:191], v[24:27]\n"
    "v_exp_f32 v38, v38\n"
    "ds_read_b128 a[8:11], v11 offset:128\n"
    "v_mfma_f32_16x16x32_bf16 v[28:31], a[28:31], v[188:191], v[20:23]\n"
    "v_add_f32 v9, v9, v38\n"
    "ds_read_b128 a[12:15], v11 offset:192\n"
    "v_mfma_f32_16x16x32_bf16 v[28:31], a[24:27], v[184:187], v[28:31]\n"
    "v_exp_f32 v39, v39\n"
    "ds_read_b128 a[28:31], v11 offset:704\n"
    "v_mfma_f32_16x16x32_bf16 v[28:31], a[20:23], v[180:183], v[28:31]\n"
    "v_add_f32 v9, v9, v39\n"
    "v_cvt_pk_bf16_f32 v47, v38, v39\n"
    "ds_read_b128 a[24:27], v11 offset:640\n"
    "v_mfma_f32_16x16x32_bf16 v[28:31], a[16:19], v[176:179], v[28:31]\n"
    "v_exp_f32 v24, v24\n"
    "ds_read_b128 a[20:23], v11 offset:576\n"
    "v_mfma_f32_16x16x32_bf16 v[80:83], a[32:35], v[44:47], 0\n"
    "v_add_f32 v10, v10, v24\n"
    "ds_read_b128 a[16:19], v11 offset:512\n"
    "s_cmp_ge_u32 s46, %[nsw]\n"
    "s_cselect_b32 s83, s79, s83\n"
    "v_mfma_f32_16x16x32_bf16 v[84:87], a[36:39], v[44:47], 0\n"
    "v_exp_f32 v25, v25\n"
    "v_mfma_f32_16x16x32_bf16 v[88:91], a[40:43], v[44:47], 0\n"
    "v_add_f32 v10, v10, v25\n"
    "v_cvt_pk_bf16_f32 v40, v24, v25\n"
    "v_mfma_f32_16x16x32_bf16 v[92:95], a[44:47], v[44:47], 0\n"
    "v_exp_f32 v26, v26\n"
    "v_mfma_f32_16x16x32_bf16 v[96:99], a[48:51], v[44:47], 0\n"
    "v_add_f32 v10, v10, v26\n"
    "v_mfma_f32_16x16x32_bf16 v[100:103], a[52:55], v[44:47], 0\n"
    "v_exp_f32 v27, v27\n"
    "v_mfma_f32_16x16x32_bf16 v[104:107], a[56:59], v[44:47], 0\n"
    "v_add_f32 v10, v10, v27\n"
    "v_cvt_pk_bf16_f32 v41, v26, v27\n"
    "v_mfma_f32_16x16x32_bf16 v[108:111], a[60:63], v[44:47], 0\n"
    "v_exp_f32 v28, v28\n"
    "s_waitcnt lgkmcnt(0)\n"
    "s_sub_u32 s81, s82, s80\n"
    "s_cmp_gt_i32 s81, 0\n"
    "s_cbranch_scc1 Ltsl_34_%=\n"
    "s_max_i32 s81, s81, 0\n"
    "s_add_u32 s82, s80, s81\n"
    "Ltb_33_%=:\n"
    "s_add_u32 s82, s82, s83\n"
    "Lloop_%=:\n"
    "v_mfma_f32_16x16x32_bf16 v[32:35], a[0:3], v[144:147], v[12:15]\n"
    "v_add_f32 v10, v10, v28\n"
    "v_mfma_f32_16x16x32_bf16 v[32:35], a[4:7], v[148:151], v[32:35]\n"
    "v_exp_f32 v29, v29\n"
    "ds_read_b32 v7, v5\n"
    "v_mfma_f32_16x16x32_bf16 v[32:35], a[8:11], v[152:155], v[32:35]\n"
    "v_add_f32 v10, v10, v29\n"
    "v_cvt_pk_bf16_f32 v42, v28, v29\n"
    "v_mfma_f32_16x16x32_bf16 v[32:35], a[12:15], v[156:159], v[32:35]\n"
    "v_exp_f32 v30, v30\n"
    "v_mfma_f32_16x16x32_bf16 v[36:39], a[28:31], v[156:159], v[12:15]\n"
    "v_add_f32 v10, v10, v30\n"
    "v_mfma_f32_16x16x32_bf16 v[36:39], a[24:27], v[152:155], v[36:39]\n"
    "v_exp_f32 v31, v31\n"
    "v_mfma_f32_16x16x32_bf16 v[36:39], a[20:23], v[148:151], v[36:39]\n"
    "v_add_f32 v10, v10, v31\n"
    "v_cvt_pk_bf16_f32 v43, v30, v31\n"
    "s_waitcnt lgkmcnt(0)\n"
    "v_cmp_gt_u32 vcc, s46, v7\n"
    "v_mfma_f32_16x16x32_bf16 v[36:39], a[16:19], v[144:147], v[36:39]\n"
    "v_exp_f32 v32, v32\n"
    "s_cbranch_vccnz Lfs_44_%=\n"
    "Lfb_43_%=:\n"
    "v_mfma_f32_16x16x32_bf16 v[112:115], a[32:35], v[40:43], v[112:115]\n"
    "v_add_f32 v8, v8, v32\n"
    "s_add_u32 m0, s40, s64\n"
    "s_xor_b32 s77, s41, 0x2000\n"
    "buffer_load_dwordx4 v3, s[20:23], s41 offen nt lds\n"
    "v_mfma_f32_16x16x32_bf16 v[116:119], a[36:39], v[40:43], v[116:119]\n"
    "v_exp_f32 v33, v33\n"
    "ds_read_b64_tr_b16 a[32:33], v2 offset:33792\n"
    "ds_read_b64_tr_b16 a[34:35], v2 offset:34304\n"
    "v_mfma_f32_16x16x32_bf16 v[120:123], a[40:43], v[40:43], v[120:123]\n"
    "v_add_f32 v8, v8, v33\n"
    "v_cvt_pk_bf16_f32 v44, v32, v33\n"
    "ds_read_b64_tr_b16 a[36:37], v2 offset:33824\n"
    "ds_read_b64_tr_b16 a[38:39], v2 offset:34336\n"
    "v_mfma_f32_16x16x32_bf16 v[124:127], a[44:47], v[40:43], v[124:127]\n"
    "v_exp_f32 v34, v34\n"
    "s_add_u32 m0, m0, 1056\n"
    "s_add_u32 s41, s41, 107520\n"
    "buffer_load_dwordx4 v3, s[20:23], s77 offen nt lds\n"
    "s_and_b32 s41, s41, 0x25cff\n"
    "ds_read_b64_tr_b16 a[40:41], v2 offset:33856\n"
    "ds_read_b64_tr_b16 a[42:43], v2 offset:34368\n"
    "v_mfma_f32_16x16x32_bf16 v[128:131], a[48:51], v[40:43], v[128:131]\n"
    "v_add_f32 v8, v8, v34\n"
    "ds_read_b64_tr_b16 a[44:45], v2 offset:33888\n"
    "ds_read_b64_tr_b16 a[46:47], v2 offset:34400\n"
    "v_mfma_f32_16x16x32_bf16 v[132:135], a[52:55], v[40:43], v[132:135]\n"
    "v_exp_f32 v35, v35\n"
    "ds_read_b64_tr_b16 a[48:49], v2 offset:33920\n"
    "ds_read_b64_tr_b16 a[50:51], v2 offset:34432\n"
    "v_mfma_f32_16x16x32_bf16 v[136:139], a[56:59], v[40:43], v[136:139]\n"
    "v_add_f32 v8, v8, v35\n"
    "v_cvt_pk_bf16_f32 v45, v34, v35\n"
    "s_add_u32 m0, s40, 42240\n"
    "s_xor_b32 s77, s42, 0x2000\n"
    "buffer_load_dwordx4 v3, s[24:27], s42 offen nt lds\n"
    "ds_read_b64_tr_b16 a[52:53], v2 offset:33952\n"
    "ds_read_b64_tr_b16 a[54:55], v2 offset:34464\n"
    "v_mfma_f32_16x16x32_bf16 v[140:143], a[60:63], v[40:43], v[140:143]\n"
    "v_exp_f32 v36, v36\n"
    "ds_read_b64_tr_b16 a[56:57], v2 offset:33984\n"
    "ds_read_b64_tr_b16 a[58:59], v2 offset:34496\n"
    "v_mfma_f32_16x16x32_bf16 v[24:27], a[0:3], v[160:163], v[16:19]\n"
    "v_add_f32 v8, v8, v36\n"
    "ds_read_b64_tr_b16 a[60:61], v2 offset:34016\n"
    "s_waitcnt lgkmcnt(14)\n"
    "ds_read_b64_tr_b16 a[62:63], v2 offset:34528\n"
    "v_mfma_f32_16x16x32_bf16 v[24:27], a[4:7], v[164:167], v[24:27]\n"
    "v_exp_f32 v37, v37\n"
    "s_add_u32 m0, m0, 1056\n"
    "s_add_u32 s42, s42, 107520\n"
    "buffer_load_dwordx4 v3, s[24:27], s77 offen nt lds\n"
    "s_and_b32 s42, s42, 0x25cff\n"
    "v_mfma_f32_16x16x32_bf16 v[24:27], a[8:11], v[168:171], v[24:27]\n"
    "v_add_f32 v8, v8, v37\n"
    "v_cvt_pk_bf16_f32 v46, v36, v37\n"
    "s_add_u32 s67, s66, 8448\n"
    "s_cmp_eq_u32 s67, 42240\n"
    "s_cselect_b32 s67, 0, s67\n"
    "v_mfma_f32_16x16x32_bf16 v[24:27], a[12:15], v[172:175], v[24:27]\n"
    "v_exp_f32 v38, v38\n"
    "v_mfma_f32_16x16x32_bf16 v[28:31], a[28:31], v[172:175], v[16:19]\n"
    "v_add_f32 v8, v8, v38\n"
    "v_mfma_f32_16x16x32_bf16 v[28:31], a[24:27], v[168:171], v[28:31]\n"
    "v_exp_f32 v39, v39\n"
    "v_mfma_f32_16x16x32_bf16 v[28:31], a[20:23], v[164:167], v[28:31]\n"
    "v_add_f32 v8, v8, v39\n"
    "v_cvt_pk_bf16_f32 v47, v38, v39\n"
    "v_mfma_f32_16x16x32_bf16 v[28:31], a[16:19], v[160:163], v[28:31]\n"
    "v_exp_f32 v24, v24\n"
    "s_waitcnt lgkmcnt(0)\n"
    "v_mfma_f32_16x16x32_bf16 v[48:51], a[32:35], v[44:47], v[48:51]\n"
    "v_add_f32 v9, v9, v24\n"
    "v_mfma_f32_16x16x32_bf16 v[52:55], a[36:39], v[44:47], v[52:55]\n"
    "v_exp_f32 v25, v25\n"
    "v_mfma_f32_16x16x32_bf16 v[56:59], a[40:43], v[44:47], v[56:59]\n"
    "v_add_f32 v9, v9, v25\n"
    "v_cvt_pk_bf16_f32 v40, v24, v25\n"
    "v_mfma_f32_16x16x32_bf16 v[60:63], a[44:47], v[44:47], v[60:63]\n"
    "v_exp_f32 v26, v26\n"
    "v_mfma_f32_16x16x32_bf16 v[64:67], a[48:51], v[44:47], v[64:67]\n"
    "v_add_f32 v9, v9, v26\n"
    "v_add_u32 v11, s66, v1\n"
    "v_mfma_f32_16x16x32_bf16 v[68:71], a[52:55], v[44:47], v[68:71]\n"
    "v_exp_f32 v27, v27\n"
    "v_mfma_f32_16x16x32_bf16 v[72:75], a[56:59], v[44:47], v[72:75]\n"
    "v_add_f32 v9, v9, v27\n"
    "v_cvt_pk_bf16_f32 v41, v26, v27\n"
    "v_mfma_f32_16x16x32_bf16 v[76:79], a[60:63], v[44:47], v[76:79]\n"
    "v_exp_f32 v28, v28\n"
    "s_waitcnt vmcnt(8)\n"
    "ds_add_u32 v4, v6\n"
    "s_add_u32 s46, s46, 1\n"
    "v_mfma_f32_16x16x32_bf16 v[32:35], a[0:3], v[176:179], v[20:23]\n"
    "v_add_f32 v9, v9, v28\n"
    "s_memrealtime s[80:81]\n"
    "v_mfma_f32_16x16x32_bf16 v[32:35], a[4:7], v[180:183], v[32:35]\n"
    "v_exp_f32 v29, v29\n"
    "ds_read_b128 a[0:3], v11 offset:0\n"
    "v_mfma_f32_16x16x32_bf16 v[32:35], a[8:11], v[184:187], v[32:35]\n"
    "v_add_f32 v9, v9, v29\n"
    "v_cvt_pk_bf16_f32 v42, v28, v29\n"
    "ds_read_b128 a[4:7], v11 offset:64\n"
    "v_mfma_f32_16x16x32_bf16 v[32:35], a[12:15], v[188:191], v[32:35]\n"
    "v_exp_f32 v30, v30\n"
    "ds_read_b128 a[8:11], v11 offset:128\n"
    "v_mfma_f32_16x16x32_bf16 v[36:39], a[28:31], v[188:191], v[20:23]\n"
    "v_add_f32 v9, v9, v30\n"
    "ds_read_b128 a[12:15], v11 offset:192\n"
    "v_mfma_f32_16x16x32_bf16 v[36:39], a[24:27], v[184:187], v[36:39]\n"
    "v_exp_f32 v31, v31\n"
    "ds_read_b128 a[28:31], v11 offset:704\n"
    "v_mfma_f32_16x16x32_bf16 v[36:39], a[20:23], v[180:183], v[36:39]\n"
    "v_add_f32 v9, v9, v31\n"
    "v_cvt_pk_bf16_f32 v43, v30, v31\n"
    "ds_read_b128 a[24:27], v11 offset:640\n"
    "v_mfma_f32_16x16x32_bf16 v[36:39], a[16:19], v[176:179], v[36:39]\n"
    "v_exp_f32 v32, v32\n"
    "ds_read_b128 a[20:23], v11 offset:576\n"
    "v_mfma_f32_16x16x32_bf16 v[80:83], a[32:35], v[40:43], v[80:83]\n"
    "v_add_f32 v10, v10, v32\n"
    "ds_read_b128 a[16:19], v11 offset:512\n"
    "s_cmp_ge_u32 s46, %[nsw]\n"
    "s_cselect_b32 s83, s79, s83\n"
    "v_mfma_f32_16x16x32_bf16 v[84:87], a[36:39], v[40:43], v[84:87]\n"
    "v_exp_f32 v33, v33\n"
    "v_mfma_f32_16x16x32_bf16 v[88:91], a[40:43], v[40:43], v[88:91]\n"
    "v_add_f32 v10, v10, v33\n"
    "v_cvt_pk_bf16_f32 v44, v32, v33\n"
    "v_mfma_f32_16x16x32_bf16 v[92:95], a[44:47], v[40:43], v[92:95]\n"
    "v_exp_f32 v34, v34\n"
    "v_mfma_f32_16x16x32_bf16 v[96:99], a[48:51], v[40:43], v[96:99]\n"
    "v_add_f32 v10, v10, v34\n"
    "v_mfma_f32_16x16x32_bf16 v[100:103], a[52:55], v[40:43], v[100:103]\n"
    "v_exp_f32 v35, v35\n"
    "v_mfma_f32_16x16x32_bf16 v[104:107], a[56:59], v[40:43], v[104:107]\n"
    "v_add_f32 v10, v10, v35\n"
    "v_cvt_pk_bf16_f32 v45, v34, v35\n"
    "v_mfma_f32_16x16x32_bf16 v[108:111], a[60:63], v[40:43], v[108:111]\n"
    "v_exp_f32 v36, v36\n"
    "s_waitcnt lgkmcnt(0)\n"
    "s_sub_u32 s81, s82, s80\n"
    "s_cmp_gt_i32 s81, 0\n"
    "s_cbranch_scc1 Ltsl_42_%=\n"
    "s_max_i32 s81, s81, 0\n"
    "s_add_u32 s82, s80, s81\n"
    "Ltb_41_%=:\n"
    "s_add_u32 s82, s82, s83\n"
    "v_mfma_f32_16x16x32_bf16 v[24:27], a[0:3], v[144:147], v[12:15]\n"
    "v_add_f32 v10, v10, v36\n"
    "v_mfma_f32_16x16x32_bf16 v[24:27], a[4:7], v[148:151], v[24:27]\n"
    "v_exp_f32 v37, v37\n"
    "ds_read_b32 v7, v5\n"
    "v_mfma_f32_16x16x32_bf16 v[24:27], a[8:11], v[152:155], v[24:27]\n"
    "v_add_f32 v10, v10, v37\n"
    "v_cvt_pk_bf16_f32 v46, v36, v37\n"
    "v_mfma_f32_16x16x32_bf16 v[24:27], a[12:15], v[156:159], v[24:27]\n"
    "v_exp_f32 v38, v38\n"
    "v_mfma_f32_16x16x32_bf16 v[28:31], a[28:31], v[156:159], v[12:15]\n"
    "v_add_f32 v10, v10, v38\n"
    "v_mfma_f32_16x16x32_bf16 v[28:31], a[24:27], v[152:155], v[28:31]\n"
    "v_exp_f32 v39, v39\n"
    "v_mfma_f32_16x16x32_bf16 v[28:31], a[20:23], v[148:151], v[28:31]\n"
    "v_add_f32 v10, v10, v39\n"
    "v_cvt_pk_bf16_f32 v47, v38, v39\n"
    "s_waitcnt lgkmcnt(0)\n"
    "v_cmp_gt_u32 vcc, s46, v7\n"
    "v_mfma_f32_16x16x32_bf16 v[28:31], a[16:19], v[144:147], v[28:31]\n"
    "v_exp_f32 v24, v24\n"
    "s_cbranch_vccnz Lfs_48_%=\n"
    "Lfb_47_%=:\n"
    "v_mfma_f32_16x16x32_bf16 v[112:115], a[32:35], v[44:47], v[112:115]\n"
    "v_add_f32 v8, v8, v24\n"
    "s_add_u32 m0, s40, s65\n"
    "s_xor_b32 s77, s41, 0x2000\n"
    "buffer_load_dwordx4 v3, s[20:23], s41 offen nt lds\n"
    "v_mfma_f32_16x16x32_bf16 v[116:119], a[36:39], v[44:47], v[116:119]\n"
    "v_exp_f32 v25, v25\n"
    "ds_read_b64_tr_b16 a[32:33], v2 offset:42240\n"
    "ds_read_b64_tr_b16 a[34:35], v2 offset:42752\n"
    "v_mfma_f32_16x16x32_bf16 v[120:123], a[40:43], v[44:47], v[120:123]\n"
    "v_add_f32 v8, v8, v25\n"
    "v_cvt_pk_bf16_f32 v40, v24, v25\n"
    "ds_read_b64_tr_b16 a[36:37], v2 offset:42272\n"
    "ds_read_b64_tr_b16 a[38:39], v2 offset:42784\n"
    "v_mfma_f32_16x16x32_bf16 v[124:127], a[44:47], v[44:47], v[124:127]\n"
    "v_exp_f32 v26, v26\n"
    "s_add_u32 m0, m0, 1056\n"
    "s_add_u32 s41, s41, 107520\n"
    "buffer_load_dwordx4 v3, s[20:23], s77 offen nt lds\n"
    "s_and_b32 s41, s41, 0x25cff\n"
    "ds_read_b64_tr_b16 a[40:41], v2 offset:42304\n"
    "ds_read_b64_tr_b16 a[42:43], v2 offset:42816\n"
    "v_mfma_f32_16x16x32_bf16 v[128:131], a[48:51], v[44:47], v[128:131]\n"
    "v_add_f32 v8, v8, v26\n"
    "ds_read_b64_tr_b16 a[44:45], v2 offset:42336\n"
    "ds_read_b64_tr_b16 a[46:47], v2 offset:42848\n"
    "v_mfma_f32_16x16x32_bf16 v[132:135], a[52:55], v[44:47], v[132:135]\n"
    "v_exp_f32 v27, v27\n"
    "ds_read_b64_tr_b16 a[48:49], v2 offset:42368\n"
    "ds_read_b64_tr_b16 a[50:51], v2 offset:42880\n"
    "v_mfma_f32_16x16x32_bf16 v[136:139], a[56:59], v[44:47], v[136:139]\n"
    "v_add_f32 v8, v8, v27\n"
    "v_cvt_pk_bf16_f32 v41, v26, v27\n"
    "s_add_u32 m0, s40, 50688\n"
    "s_xor_b32 s77, s42, 0x2000\n"
    "buffer_load_dwordx4 v3, s[24:27], s42 offen nt lds\n"
    "ds_read_b64_tr_b16 a[52:53], v2 offset:42400\n"
    "ds_read_b64_tr_b16 a[54:55], v2 offset:42912\n"
    "v_mfma_f32_16x16x32_bf16 v[140:143], a[60:63], v[44:47], v[140:143]\n"
    "v_exp_f32 v28, v28\n"
    "ds_read_b64_tr_b16 a[56:57], v2 offset:42432\n"
    "ds_read_b64_tr_b16 a[58:59], v2 offset:42944\n"
    "v_mfma_f32_16x16x32_bf16 v[32:35], a[0:3], v[160:163], v[16:19]\n"
    "v_add_f32 v8, v8, v28\n"
    "ds_read_b64_tr_b16 a[60:61], v2 offset:42464\n"
    "s_waitcnt lgkmcnt(14)\n"
    "ds_read_b64_tr_b16 a[62:63], v2 offset:42976\n"
    "v_mfma_f32_16x16x32_bf16 v[32:35], a[4:7], v[164:167], v[32:35]\n"
    "v_exp_f32 v29, v29\n"
    "s_add_u32 m0, m0, 1056\n"
    "s_add_u32 s42, s42, 107520\n"
    "buffer_load_dwordx4 v3, s[24:27], s77 offen nt lds\n"
    "s_and_b32 s42, s42, 0x25cff\n"
    "v_mfma_f32_16x16x32_bf16 v[32:35], a[8:11], v[168:171], v[32:35]\n"
    "v_add_f32 v8, v8, v29\n"
    "v_cvt_pk_bf16_f32 v42, v28, v29\n"
    "s_add_u32 s64, s67, 8448\n"
    "s_cmp_eq_u32 s64, 42240\n"
    "s_cselect_b32 s64, 0, s64\n"
    "v_mfma_f32_16x16x32_bf16 v[32:35], a[12:15], v[172:175], v[32:35]\n"
    "v_exp_f32 v30, v30\n"
    "v_mfma_f32_16x16x32_bf16 v[36:39], a[28:31], v[172:175], v[16:19]\n"
    "v_add_f32 v8, v8, v30\n"
    "v_mfma_f32_16x16x32_bf16 v[36:39], a[24:27], v[168:171], v[36:39]\n"
    "v_exp_f32 v31, v31\n"
    "v_mfma_f32_16x16x32_bf16 v[36:39], a[20:23], v[164:167], v[36:39]\n"
    "v_add_f32 v8, v8, v31\n"
    "v_cvt_pk_bf16_f32 v43, v30, v31\n"
    "v_mfma_f32_16x16x32_bf16 v[36:39], a[16:19], v[160:163], v[36:39]\n"
    "v_exp_f32 v32, v32\n"
    "s_waitcnt lgkmcnt(0)\n"
    "v_mfma_f32_16x16x32_bf16 v[48:51], a[32:35], v[40:43], v[48:51]\n"
    "v_add_f32 v9, v9, v32\n"
    "v_mfma_f32_16x16x32_bf16 v[52:55], a[36:39], v[40:43], v[52:55]\n"
    "v_exp_f32 v33, v33\n"
    "v_mfma_f32_16x16x32_bf16 v[56:59], a[40:43], v[40:43], v[56:59]\n"
    "v_add_f32 v9, v9, v33\n"
    "v_cvt_pk_bf16_f32 v44, v32, v33\n"
    "v_mfma_f32_16x16x32_bf16 v[60:63], a[44:47], v[40:43], v[60:63]\n"
    "v_exp_f32 v34, v34\n"
    "v_mfma_f32_16x16x32_bf16 v[64:67], a[48:51], v[40:43], v[64:67]\n"
    "v_add_f32 v9, v9, v34\n"
    "v_add_u32 v11, s67, v1\n"
    "v_mfma_f32_16x16x32_bf16 v[68:71], a[52:55], v[40:43], v[68:71]\n"
    "v_exp_f32 v35, v35\n"
    "v_mfma_f32_16x16x32_bf16 v[72:75], a[56:59], v[40:43], v[72:75]\n"
    "v_add_f32 v9, v9, v35\n"
    "v_cvt_pk_bf16_f32 v45, v34, v35\n"
    "v_mfma_f32_16x16x32_bf16 v[76:79], a[60:63], v[40:43], v[76:79]\n"
    "v_exp_f32 v36, v36\n"
    "s_waitcnt vmcnt(8)\n"
    "ds_add_u32 v4, v6\n"
    "s_add_u32 s46, s46, 1\n"
    "v_mfma_f32_16x16x32_bf16 v[24:27], a[0:3], v[176:179], v[20:23]\n"
    "v_add_f32 v9, v9, v36\n"
    "s_memrealtime s[80:81]\n"
    "v_mfma_f32_16x16x32_bf16 v[24:27], a[4:7], v[180:183], v[24:27]\n"
    "v_exp_f32 v37, v37\n"
    "ds_read_b128 a[0:3], v11 offset:0\n"
    "v_mfma_f32_16x16x32_bf16 v[24:27], a[8:11], v[184:187], v[24:27]\n"
    "v_add_f32 v9, v9, v37\n"
    "v_cvt_pk_bf16_f32 v46, v36, v37\n"
    "ds_read_b128 a[4:7], v11 offset:64\n"
    "v_mfma_f32_16x16x32_bf16 v[24:27], a[12:15], v[188:191], v[24:27]\n"
    "v_exp_f32 v38, v38\n"
    "ds_read_b128 a[8:11], v11 offset:128\n"
    "v_mfma_f32_16x16x32_bf16 v[28:31], a[28:31], v[188:191], v[20:23]\n"
    "v_add_f32 v9, v9, v38\n"
    "ds_read_b128 a[12:15], v11 offset:192\n"
    "v_mfma_f32_16x16x32_bf16 v[28:31], a[24:27], v[184:187], v[28:31]\n"
    "v_exp_f32 v39, v39\n"
    "ds_read_b128 a[28:31], v11 offset:704\n"
    "v_mfma_f32_16x16x32_bf16 v[28:31], a[20:23], v[180:183], v[28:31]\n"
    "v_add_f32 v9, v9, v39\n"
    "v_cvt_pk_bf16_f32 v47, v38, v39\n"
    "ds_read_b128 a[24:27], v11 offset:640\n"
    "v_mfma_f32_16x16x32_bf16 v[28:31], a[16:19], v[176:179], v[28:31]\n"
    "v_exp_f32 v24, v24\n"
    "ds_read_b128 a[20:23], v11 offset:576\n"
    "v_mfma_f32_16x16x32_bf16 v[80:83], a[32:35], v[44:47], v[80:83]\n"
    "v_add_f32 v10, v10, v24\n"
    "ds_read_b128 a[16:19], v11 offset:512\n"
    "s_cmp_ge_u32 s46, %[nsw]\n"
    "s_cselect_b32 s83, s79, s83\n"
    "v_mfma_f32_16x16x32_bf16 v[84:87], a[36:39], v[44:47], v[84:87]\n"
    "v_exp_f32 v25, v25\n"
    "v_mfma_f32_16x16x32_bf16 v[88:91], a[40:43], v[44:47], v[88:91]\n"
    "v_add_f32 v10, v10, v25\n"
    "v_cvt_pk_bf16_f32 v40, v24, v25\n"
    "v_mfma_f32_16x16x32_bf16 v[92:95], a[44:47], v[44:47], v[92:95]\n"
    "v_exp_f32 v26, v26\n"
    "v_mfma_f32_16x16x32_bf16 v[96:99], a[48:51], v[44:47], v[96:99]\n"
    "v_add_f32 v10, v10, v26\n"
    "v_mfma_f32_16x16x32_bf16 v[100:103], a[52:55], v[44:47], v[100:103]\n"
    "v_exp_f32 v27, v27\n"
    "v_mfma_f32_16x16x32_bf16 v[104:107], a[56:59], v[44:47], v[104:107]\n"
    "v_add_f32 v10, v10, v27\n"
    "v_cvt_pk_bf16_f32 v41, v26, v27\n"
    "v_mfma_f32_16x16x32_bf16 v[108:111], a[60:63], v[44:47], v[108:111]\n"
    "v_exp_f32 v28, v28\n"
    "s_waitcnt lgkmcnt(0)\n"
    "s_sub_u32 s81, s82, s80\n"
    "s_cmp_gt_i32 s81, 0\n"
    "s_cbranch_scc1 Ltsl_46_%=\n"
    "s_max_i32 s81, s81, 0\n"
    "s_add_u32 s82, s80, s81\n"
    "Ltb_45_%=:\n"
    "s_add_u32 s82, s82, s83\n"
    "v_mfma_f32_16x16x32_bf16 v[32:35], a[0:3], v[144:147], v[12:15]\n"
    "v_add_f32 v10, v10, v28\n"
    "v_mfma_f32_16x16x32_bf16 v[32:35], a[4:7], v[148:151], v[32:35]\n"
    "v_exp_f32 v29, v29\n"
    "ds_read_b32 v7, v5\n"
    "v_mfma_f32_16x16x32_bf16 v[32:35], a[8:11], v[152:155], v[32:35]\n"
    "v_add_f32 v10, v10, v29\n"
    "v_cvt_pk_bf16_f32 v42, v28, v29\n"
    "v_mfma_f32_16x16x32_bf16 v[32:35], a[12:15], v[156:159], v[32:35]\n"
    "v_exp_f32 v30, v30\n"
    "v_mfma_f32_16x16x32_bf16 v[36:39], a[28:31], v[156:159], v[12:15]\n"
    "v_add_f32 v10, v10, v30\n"
    "v_mfma_f32_16x16x32_bf16 v[36:39], a[24:27], v[152:155], v[36:39]\n"
    "v_exp_f32 v31, v31\n"
    "v_mfma_f32_16x16x32_bf16 v[36:39], a[20:23], v[148:151], v[36:39]\n"
    "v_add_f32 v10, v10, v31\n"
    "v_cvt_pk_bf16_f32 v43, v30, v31\n"
    "s_waitcnt lgkmcnt(0)\n"
    "v_cmp_gt_u32 vcc, s46, v7\n"
    "v_mfma_f32_16x16x32_bf16 v[36:39], a[16:19], v[144:147], v[36:39]\n"
    "v_exp_f32 v32, v32\n"
    "s_cbranch_vccnz Lfs_52_%=\n"
    "Lfb_51_%=:\n"
    "v_mfma_f32_16x16x32_bf16 v[112:115], a[32:35], v[40:43], v[112:115]\n"
    "v_add_f32 v8, v8, v32\n"
    "s_add_u32 m0, s40, s66\n"
    "s_xor_b32 s77, s41, 0x2000\n"
    "buffer_load_dwordx4 v3, s[20:23], s41 offen nt lds\n"
    "v_mfma_f32_16x16x32_bf16 v[116:119], a[36:39], v[40:43], v[116:119]\n"
    "v_exp_f32 v33, v33\n"
    "ds_read_b64_tr_b16 a[32:33], v2 offset:50688\n"
    "ds_read_b64_tr_b16 a[34:35], v2 offset:51200\n"
    "v_mfma_f32_16x16x32_bf16 v[120:123], a[40:43], v[40:43], v[120:123]\n"
    "v_add_f32 v8, v8, v33\n"
    "v_cvt_pk_bf16_f32 v44, v32, v33\n"
    "ds_read_b64_tr_b16 a[36:37], v2 offset:50720\n"
    "ds_read_b64_tr_b16 a[38:39], v2 offset:51232\n"
    "v_mfma_f32_16x16x32_bf16 v[124:127], a[44:47], v[40:43], v[124:127]\n"
    "v_exp_f32 v34, v34\n"
    "s_add_u32 m0, m0, 1056\n"
    "s_add_u32 s41, s41, 107520\n"
    "buffer_load_dwordx4 v3, s[20:23], s77 offen nt lds\n"
    "s_and_b32 s41, s41, 0x25cff\n"
    "ds_read_b64_tr_b16 a[40:41], v2 offset:50752\n"
    "ds_read_b64_tr_b16 a[42:43], v2 offset:51264\n"
    "v_mfma_f32_16x16x32_bf16 v[128:131], a[48:51], v[40:43], v[128:131]\n"
    "v_add_f32 v8, v8, v34\n"
    "ds_read_b64_tr_b16 a[44:45], v2 offset:50784\n"
    "ds_read_b64_tr_b16 a[46:47], v2 offset:51296\n"
    "v_mfma_f32_16x16x32_bf16 v[132:135], a[52:55], v[40:43], v[132:135]\n"
    "v_exp_f32 v35, v35\n"
    "ds_read_b64_tr_b16 a[48:49], v2 offset:50816\n"
    "ds_read_b64_tr_b16 a[50:51], v2 offset:51328\n"
    "v_mfma_f32_16x16x32_bf16 v[136:139], a[56:59], v[40:43], v[136:139]\n"
    "v_add_f32 v8, v8, v35\n"
    "v_cvt_pk_bf16_f32 v45, v34, v35\n"
    "s_add_u32 m0, s40, 59136\n"
    "s_xor_b32 s77, s42, 0x2000\n"
    "buffer_load_dwordx4 v3, s[24:27], s42 offen nt lds\n"
    "ds_read_b64_tr_b16 a[52:53], v2 offset:50848\n"
    "ds_read_b64_tr_b16 a[54:55], v2 offset:51360\n"
    "v_mfma_f32_16x16x32_bf16 v[140:143], a[60:63], v[40:43], v[140:143]\n"
    "v_exp_f32 v36, v36\n"
    "ds_read_b64_tr_b16 a[56:57], v2 offset:50880\n"
    "ds_read_b64_tr_b16 a[58:59], v2 offset:51392\n"
    "v_mfma_f32_16x16x32_bf16 v[24:27], a[0:3], v[160:163], v[16:19]\n"
    "v_add_f32 v8, v8, v36\n"
    "ds_read_b64_tr_b16 a[60:61], v2 offset:50912\n"
    "s_waitcnt lgkmcnt(14)\n"
    "ds_read_b64_tr_b16 a[62:63], v2 offset:51424\n"
    "v_mfma_f32_16x16x32_bf16 v[24:27], a[4:7], v[164:167], v[24:27]\n"
    "v_exp_f32 v37, v37\n"
    "s_add_u32 m0, m0, 1056\n"
    "s_add_u32 s42, s42, 107520\n"
    "buffer_load_dwordx4 v3, s[24:27], s77 offen nt lds\n"
    "s_and_b32 s42, s42, 0x25cff\n"
    "v_mfma_f32_16x16x32_bf16 v[24:27], a[8:11], v[168:171], v[24:27]\n"
    "v_add_f32 v8, v8, v37\n"
    "v_cvt_pk_bf16_f32 v46, v36, v37\n"
    "s_add_u32 s65, s64, 8448\n"
    "s_cmp_eq_u32 s65, 42240\n"
    "s_cselect_b32 s65, 0, s65\n"
    "v_mfma_f32_16x16x32_bf16 v[24:27], a[12:15], v[172:175], v[24:27]\n"
    "v_exp_f32 v38, v38\n"
    "v_mfma_f32_16x16x32_bf16 v[28:31], a[28:31], v[172:175], v[16:19]\n"
    "v_add_f32 v8, v8, v38\n"
    "v_mfma_f32_16x16x32_bf16 v[28:31], a[24:27], v[168:171], v[28:31]\n"
    "v_exp_f32 v39, v39\n"
    "v_mfma_f32_16x16x32_bf16 v[28:31], a[20:23], v[164:167], v[28:31]\n"
    "v_add_f32 v8, v8, v39\n"
    "v_cvt_pk_bf16_f32 v47, v38, v39\n"
    "v_mfma_f32_16x16x32_bf16 v[28:31], a[16:19], v[160:163], v[28:31]\n"
    "v_exp_f32 v24, v24\n"
    "s_waitcnt lgkmcnt(0)\n"
    "v_mfma_f32_16x16x32_bf16 v[48:51], a[32:35], v[44:47], v[48:51]\n"
    "v_add_f32 v9, v9, v24\n"
    "v_mfma_f32_16x16x32_bf16 v[52:55], a[36:39], v[44:47], v[52:55]\n"
    "v_exp_f32 v25, v25\n"
    "v_mfma_f32_16x16x32_bf16 v[56:59], a[40:43], v[44:47], v[56:59]\n"
    "v_add_f32 v9, v9, v25\n"
    "v_cvt_pk_bf16_f32 v40, v24, v25\n"
    "v_mfma_f32_16x16x32_bf16 v[60:63], a[44:47], v[44:47], v[60:63]\n"
    "v_exp_f32 v26, v26\n"
    "v_mfma_f32_16x16x32_bf16 v[64:67], a[48:51], v[44:47], v[64:67]\n"
    "v_add_f32 v9, v9, v26\n"
    "v_add_u32 v11, s64, v1\n"
    "v_mfma_f32_16x16x32_bf16 v[68:71], a[52:55], v[44:47], v[68:71]\n"
    "v_exp_f32 v27, v27\n"
    "v_mfma_f32_16x16x32_bf16 v[72:75], a[56:59], v[44:47], v[72:75]\n"
    "v_add_f32 v9, v9, v27\n"
    "v_cvt_pk_bf16_f32 v41, v26, v27\n"
    "v_mfma_f32_16x16x32_bf16 v[76:79], a[60:63], v[44:47], v[76:79]\n"
    "v_exp_f32 v28, v28\n"
    "s_waitcnt vmcnt(8)\n"
    "ds_add_u32 v4, v6\n"
    "s_add_u32 s46, s46, 1\n"
    "v_mfma_f32_16x16x32_bf16 v[32:35], a[0:3], v[176:179], v[20:23]\n"
    "v_add_f32 v9, v9, v28\n"
    "s_memrealtime s[80:81]\n"
    "v_mfma_f32_16x16x32_bf16 v[32:35], a[4:7], v[180:183], v[32:35]\n"
    "v_exp_f32 v29, v29\n"
    "ds_read_b128 a[0:3], v11 offset:0\n"
    "v_mfma_f32_16x16x32_bf16 v[32:35], a[8:11], v[184:187], v[32:35]\n"
    "v_add_f32 v9, v9, v29\n"
    "v_cvt_pk_bf16_f32 v42, v28, v29\n"
    "ds_read_b128 a[4:7], v11 offset:64\n"
    "v_mfma_f32_16x16x32_bf16 v[32:35], a[12:15], v[188:191], v[32:35]\n"
    "v_exp_f32 v30, v30\n"
    "ds_read_b128 a[8:11], v11 offset:128\n"
    "v_mfma_f32_16x16x32_bf16 v[36:39], a[28:31], v[188:191], v[20:23]\n"
    "v_add_f32 v9, v9, v30\n"
    "ds_read_b128 a[12:15], v11 offset:192\n"
    "v_mfma_f32_16x16x32_bf16 v[36:39], a[24:27], v[184:187], v[36:39]\n"
    "v_exp_f32 v31, v31\n"
    "ds_read_b128 a[28:31], v11 offset:704\n"
    "v_mfma_f32_16x16x32_bf16 v[36:39], a[20:23], v[180:183], v[36:39]\n"
    "v_add_f32 v9, v9, v31\n"
    "v_cvt_pk_bf16_f32 v43, v30, v31\n"
    "ds_read_b128 a[24:27], v11 offset:640\n"
    "v_mfma_f32_16x16x32_bf16 v[36:39], a[16:19], v[176:179], v[36:39]\n"
    "v_exp_f32 v32, v32\n"
    "ds_read_b128 a[20:23], v11 offset:576\n"
    "v_mfma_f32_16x16x32_bf16 v[80:83], a[32:35], v[40:43], v[80:83]\n"
    "v_add_f32 v10, v10, v32\n"
    "ds_read_b128 a[16:19], v11 offset:512\n"
    "s_cmp_ge_u32 s46, %[nsw]\n"
    "s_cselect_b32 s83, s79, s83\n"
    "v_mfma_f32_16x16x32_bf16 v[84:87], a[36:39], v[40:43], v[84:87]\n"
    "v_exp_f32 v33, v33\n"
    "v_mfma_f32_16x16x32_bf16 v[88:91], a[40:43], v[40:43], v[88:91]\n"
    "v_add_f32 v10, v10, v33\n"
    "v_cvt_pk_bf16_f32 v44, v32, v33\n"
    "v_mfma_f32_16x16x32_bf16 v[92:95], a[44:47], v[40:43], v[92:95]\n"
    "v_exp_f32 v34, v34\n"
    "v_mfma_f32_16x16x32_bf16 v[96:99], a[48:51], v[40:43], v[96:99]\n"
    "v_add_f32 v10, v10, v34\n"
    "v_mfma_f32_16x16x32_bf16 v[100:103], a[52:55], v[40:43], v[100:103]\n"
    "v_exp_f32 v35, v35\n"
    "v_mfma_f32_16x16x32_bf16 v[104:107], a[56:59], v[40:43], v[104:107]\n"
    "v_add_f32 v10, v10, v35\n"
    "v_cvt_pk_bf16_f32 v45, v34, v35\n"
    "v_mfma_f32_16x16x32_bf16 v[108:111], a[60:63], v[40:43], v[108:111]\n"
    "v_exp_f32 v36, v36\n"
    "s_waitcnt lgkmcnt(0)\n"
    "s_sub_u32 s81, s82, s80\n"
    "s_cmp_gt_i32 s81, 0\n"
    "s_cbranch_scc1 Ltsl_50_%=\n"
    "s_max_i32 s81, s81, 0\n"
    "s_add_u32 s82, s80, s81\n"
    "Ltb_49_%=:\n"
    "s_add_u32 s82, s82, s83\n"
    "s_sub_u32 s45, s45, 1\n"
    "s_cmp_eq_u32 s45, 0\n"
    "s_cbranch_scc1 Lexit_%=\n"
    "Lmid_%=:\n"
    "v_mfma_f32_16x16x32_bf16 v[24:27], a[0:3], v[144:147], v[12:15]\n"
    "v_add_f32 v10, v10, v36\n"
    "v_mfma_f32_16x16x32_bf16 v[24:27], a[4:7], v[148:151], v[24:27]\n"
    "v_exp_f32 v37, v37\n"
    "ds_read_b32 v7, v5\n"
    "v_mfma_f32_16x16x32_bf16 v[24:27], a[8:11], v[152:155], v[24:27]\n"
    "v_add_f32 v10, v10, v37\n"
    "v_cvt_pk_bf16_f32 v46, v36, v37\n"
    "v_mfma_f32_16x16x32_bf16 v[24:27], a[12:15], v[156:159], v[24:27]\n"
    "v_exp_f32 v38, v38\n"
    "v_mfma_f32_16x16x32_bf16 v[28:31], a[28:31], v[156:159], v[12:15]\n"
    "v_add_f32 v10, v10, v38\n"
    "v_mfma_f32_16x16x32_bf16 v[28:31], a[24:27], v[152:155], v[28:31]\n"
    "v_exp_f32 v39, v39\n"
    "v_mfma_f32_16x16x32_bf16 v[28:31], a[20:23], v[148:151], v[28:31]\n"
    "v_add_f32 v10, v10, v39\n"
    "v_cvt_pk_bf16_f32 v47, v38, v39\n"
    "s_waitcnt lgkmcnt(0)\n"
    "v_cmp_gt_u32 vcc, s46, v7\n"
    "v_mfma_f32_16x16x32_bf16 v[28:31], a[16:19], v[144:147], v[28:31]\n"
    "v_exp_f32 v24, v24\n"
    "s_cbranch_vccnz Lfs_56_%=\n"
    "Lfb_55_%=:\n"
    "v_mfma_f32_16x16x32_bf16 v[112:115], a[32:35], v[44:47], v[112:115]\n"
    "v_add_f32 v8, v8, v24\n"
    "s_add_u32 m0, s40, s67\n"
    "s_xor_b32 s77, s41, 0x2000\n"
    "buffer_load_dwordx4 v3, s[20:23], s41 offen nt lds\n"
    "v_mfma_f32_16x16x32_bf16 v[116:119], a[36:39], v[44:47], v[116:119]\n"
    "v_exp_f32 v25, v25\n"
    "ds_read_b64_tr_b16 a[32:33], v2 offset:25344\n"
    "ds_read_b64_tr_b16 a[34:35], v2 offset:25856\n"
    "v_mfma_f32_16x16x32_bf16 v[120:123], a[40:43], v[44:47], v[120:123]\n"
    "v_add_f32 v8, v8, v25\n"
    "v_cvt_pk_bf16_f32 v40, v24, v25\n"
    "ds_read_b64_tr_b16 a[36:37], v2 offset:25376\n"
    "ds_read_b64_tr_b16 a[38:39], v2 offset:25888\n"
    "v_mfma_f32_16x16x32_bf16 v[124:127], a[44:47], v[44:47], v[124:127]\n"
    "v_exp_f32 v26, v26\n"
    "s_add_u32 m0, m0, 1056\n"
    "s_add_u32 s41, s41, 107520\n"
    "buffer_load_dwordx4 v3, s[20:23], s77 offen nt lds\n"
    "s_and_b32 s41, s41, 0x25cff\n"
    "ds_read_b64_tr_b16 a[40:41], v2 offset:25408\n"
    "ds_read_b64_tr_b16 a[42:43], v2 offset:25920\n"
    "v_mfma_f32_16x16x32_bf16 v[128:131], a[48:51], v[44:47], v[128:131]\n"
    "v_add_f32 v8, v8, v26\n"
    "ds_read_b64_tr_b16 a[44:45], v2 offset:25440\n"
    "ds_read_b64_tr_b16 a[46:47], v2 offset:25952\n"
    "v_mfma_f32_16x16x32_bf16 v[132:135], a[52:55], v[44:47], v[132:135]\n"
    "v_exp_f32 v27, v27\n"
    "ds_read_b64_tr_b16 a[48:49], v2 offset:25472\n"
    "ds_read_b64_tr_b16 a[50:51], v2 offset:25984\n"
    "v_mfma_f32_16x16x32_bf16 v[136:139], a[56:59], v[44:47], v[136:139]\n"
    "v_add_f32 v8, v8, v27\n"
    "v_cvt_pk_bf16_f32 v41, v26, v27\n"
    "s_add_u32 m0, s40, 67584\n"
    "s_xor_b32 s77, s42, 0x2000\n"
    "buffer_load_dwordx4 v3, s[24:27], s42 offen nt lds\n"
    "ds_read_b64_tr_b16 a[52:53], v2 offset:25504\n"
    "ds_read_b64_tr_b16 a[54:55], v2 offset:26016\n"
    "v_mfma_f32_16x16x32_bf16 v[140:143], a[60:63], v[44:47], v[140:143]\n"
    "v_exp_f32 v28, v28\n"
    "ds_read_b64_tr_b16 a[56:57], v2 offset:25536\n"
    "ds_read_b64_tr_b16 a[58:59], v2 offset:26048\n"
    "v_mfma_f32_16x16x32_bf16 v[32:35], a[0:3], v[160:163], v[16:19]\n"
    "v_add_f32 v8, v8, v28\n"
    "ds_read_b64_tr_b16 a[60:61], v2 offset:25568\n"
    "s_waitcnt lgkmcnt(14)\n"
    "ds_read_b64_tr_b16 a[62:63], v2 offset:26080\n"
    "v_mfma_f32_16x16x32_bf16 v[32:35], a[4:7], v[164:167], v[32:35]\n"
    "v_exp_f32 v29, v29\n"
    "s_add_u32 m0, m0, 1056\n"
    "s_add_u32 s42, s42, 107520\n"
    "buffer_load_dwordx4 v3, s[24:27], s77 offen nt lds\n"
    "s_and_b32 s42, s42, 0x25cff\n"
    "v_mfma_f32_16x16x32_bf16 v[32:35], a[8:11], v[168:171], v[32:35]\n"
    "v_add_f32 v8, v8, v29\n"
    "v_cvt_pk_bf16_f32 v42, v28, v29\n"
    "s_add_u32 s66, s65, 8448\n"
    "s_cmp_eq_u32 s66, 42240\n"
    "s_cselect_b32 s66, 0, s66\n"
    "v_mfma_f32_16x16x32_bf16 v[32:35], a[12:15], v[172:175], v[32:35]\n"
    "v_exp_f32 v30, v30\n"
    "v_mfma_f32_16x16x32_bf16 v[36:39], a[28:31], v[172:175], v[16:19]\n"
    "v_add_f32 v8, v8, v30\n"
    "v_mfma_f32_16x16x32_bf16 v[36:39], a[24:27], v[168:171], v[36:39]\n"
    "v_exp_f32 v31, v31\n"
    "v_mfma_f32_16x16x32_bf16 v[36:39], a[20:23], v[164:167], v[36:39]\n"
    "v_add_f32 v8, v8, v31\n"
    "v_cvt_pk_bf16_f32 v43, v30, v31\n"
    "v_mfma_f32_16x16x32_bf16 v[36:39], a[16:19], v[160:163], v[36:39]\n"
    "v_exp_f32 v32, v32\n"
    "s_waitcnt lgkmcnt(0)\n"
    "v_mfma_f32_16x16x32_bf16 v[48:51], a[32:35], v[40:43], v[48:51]\n"
    "v_add_f32 v9, v9, v32\n"
    "v_mfma_f32_16x16x32_bf16 v[52:55], a[36:39], v[40:43], v[52:55]\n"
    "v_exp_f32 v33, v33\n"
    "v_mfma_f32_16x16x32_bf16 v[56:59], a[40:43], v[40:43], v[56:59]\n"
    "v_add_f32 v9, v9, v33\n"
    "v_cvt_pk_bf16_f32 v44, v32, v33\n"
    "v_mfma_f32_16x16x32_bf16 v[60:63], a[44:47], v[40:43], v[60:63]\n"
    "v_exp_f32 v34, v34\n"
    "v_mfma_f32_16x16x32_bf16 v[64:67], a[48:51], v[40:43], v[64:67]\n"
    "v_add_f32 v9, v9, v34\n"
    "v_add_u32 v11, s65, v1\n"
    "v_mfma_f32_16x16x32_bf16 v[68:71], a[52:55], v[40:43], v[68:71]\n"
    "v_exp_f32 v35, v35\n"
    "v_mfma_f32_16x16x32_bf16 v[72:75], a[56:59], v[40:43], v[72:75]\n"
    "v_add_f32 v9, v9, v35\n"
    "v_cvt_pk_bf16_f32 v45, v34, v35\n"
    "v_mfma_f32_16x16x32_bf16 v[76:79], a[60:63], v[40:43], v[76:79]\n"
    "v_exp_f32 v36, v36\n"
    "s_waitcnt vmcnt(8)\n"
    "ds_add_u32 v4, v6\n"
    "s_add_u32 s46, s46, 1\n"
    "v_mfma_f32_16x16x32_bf16 v[24:27], a[0:3], v[176:179], v[20:23]\n"
    "v_add_f32 v9, v9, v36\n"
    "s_memrealtime s[80:81]\n"
    "v_mfma_f32_16x16x32_bf16 v[24:27], a[4:7], v[180:183], v[24:27]\n"
    "v_exp_f32 v37, v37\n"
    "ds_read_b128 a[0:3], v11 offset:0\n"
    "v_mfma_f32_16x16x32_bf16 v[24:27], a[8:11], v[184:187], v[24:27]\n"
    "v_add_f32 v9, v9, v37\n"
    "v_cvt_pk_bf16_f32 v46, v36, v37\n"
    "ds_read_b128 a[4:7], v11 offset:64\n"
    "v_mfma_f32_16x16x32_bf16 v[24:27], a[12:15], v[188:191], v[24:27]\n"
    "v_exp_f32 v38, v38\n"
    "ds_read_b128 a[8:11], v11 offset:128\n"
    "v_mfma_f32_16x16x32_bf16 v[28:31], a[28:31], v[188:191], v[20:23]\n"
    "v_add_f32 v9, v9, v38\n"
    "ds_read_b128 a[12:15], v11 offset:192\n"
    "v_mfma_f32_16x16x32_bf16 v[28:31], a[24:27], v[184:187], v[28:31]\n"
    "v_exp_f32 v39, v39\n"
    "ds_read_b128 a[28:31], v11 offset:704\n"
    "v_mfma_f32_16x16x32_bf16 v[28:31], a[20:23], v[180:183], v[28:31]\n"
    "v_add_f32 v9, v9, v39\n"
    "v_cvt_pk_bf16_f32 v47, v38, v39\n"
    "ds_read_b128 a[24:27], v11 offset:640\n"
    "v_mfma_f32_16x16x32_bf16 v[28:31], a[16:19], v[176:179], v[28:31]\n"
    "v_exp_f32 v24, v24\n"
    "ds_read_b128 a[20:23], v11 offset:576\n"
    "v_mfma_f32_16x16x32_bf16 v[80:83], a[32:35], v[44:47], v[80:83]\n"
    "v_add_f32 v10, v10, v24\n"
    "ds_read_b128 a[16:19], v11 offset:512\n"
    "s_cmp_ge_u32 s46, %[nsw]\n"
    "s_cselect_b32 s83, s79, s83\n"
    "v_mfma_f32_16x16x32_bf16 v[84:87], a[36:39], v[44:47], v[84:87]\n"
    "v_exp_f32 v25, v25\n"
    "v_mfma_f32_16x16x32_bf16 v[88:91], a[40:43], v[44:47], v[88:91]\n"
    "v_add_f32 v10, v10, v25\n"
    "v_cvt_pk_bf16_f32 v40, v24, v25\n"
    "v_mfma_f32_16x16x32_bf16 v[92:95], a[44:47], v[44:47], v[92:95]\n"
    "v_exp_f32 v26, v26\n"
    "v_mfma_f32_16x16x32_bf16 v[96:99], a[48:51], v[44:47], v[96:99]\n"
    "v_add_f32 v10, v10, v26\n"
    "v_mfma_f32_16x16x32_bf16 v[100:103], a[52:55], v[44:47], v[100:103]\n"
    "v_exp_f32 v27, v27\n"
    "v_mfma_f32_16x16x32_bf16 v[104:107], a[56:59], v[44:47], v[104:107]\n"
    "v_add_f32 v10, v10, v27\n"
    "v_cvt_pk_bf16_f32 v41, v26, v27\n"
    "v_mfma_f32_16x16x32_bf16 v[108:111], a[60:63], v[44:47], v[108:111]\n"
    "v_exp_f32 v28, v28\n"
    "s_waitcnt lgkmcnt(0)\n"
    "s_sub_u32 s81, s82, s80\n"
    "s_cmp_gt_i32 s81, 0\n"
    "s_cbranch_scc1 Ltsl_54_%=\n"
    "s_max_i32 s81, s81, 0\n"
    "s_add_u32 s82, s80, s81\n"
    "Ltb_53_%=:\n"
    "s_add_u32 s82, s82, s83\n"
    "s_branch Lloop_%=\n"
    "Lexit_%=:\n"
    "s_waitcnt lgkmcnt(0)\n"
    "s_cmp_lg_u32 %[team], 0\n"
    "s_cselect_b32 s79, 10, s79\n"
    "s_cbranch_scc1 Ltp_%=\n"
    "s_setprio 0\n"
    "Ltp_%=:\n"
    "v_mfma_f32_16x16x32_bf16 v[24:27], a[0:3], v[144:147], v[12:15]\n"
    "v_add_f32 v10, v10, v36\n"
    "s_mov_b64 s[28:29], %[wp2]\n"
    "s_and_b32 s29, s29, 0xffff\n"
    "v_mfma_f32_16x16x32_bf16 v[24:27], a[4:7], v[148:151], v[24:27]\n"
    "v_exp_f32 v37, v37\n"
    "s_mov_b32 s30, %[wsz2]\n"
    "s_mov_b32 s31, 0x20000\n"
    "ds_read_b32 v7, v5\n"
    "v_mfma_f32_16x16x32_bf16 v[24:27], a[8:11], v[152:155], v[24:27]\n"
    "v_add_f32 v10, v10, v37\n"
    "v_cvt_pk_bf16_f32 v46, v36, v37\n"
    "s_mov_b32 s48, %[htab]\n"
    "s_bfe_u32 s41, s48, 196608\n"
    "v_mfma_f32_16x16x32_bf16 v[24:27], a[12:15], v[156:159], v[24:27]\n"
    "v_exp_f32 v38, v38\n"
    "s_lshl_b32 s43, s41, 17\n"
    "s_bfe_u32 s54, %[inbox], 0x2000a\n"
    "v_mfma_f32_16x16x32_bf16 v[28:31], a[28:31], v[156:159], v[12:15]\n"
    "v_add_f32 v10, v10, v38\n"
    "s_lshl_b32 s54, s54, 6\n"
    "s_lshl_b32 s55, %[team], 5\n"
    "v_mfma_f32_16x16x32_bf16 v[28:31], a[24:27], v[152:155], v[28:31]\n"
    "v_exp_f32 v39, v39\n"
    "v_mfma_f32_16x16x32_bf16 v[28:31], a[20:23], v[148:151], v[28:31]\n"
    "v_add_f32 v10, v10, v39\n"
    "v_cvt_pk_bf16_f32 v47, v38, v39\n"
    "s_waitcnt lgkmcnt(0)\n"
    "v_cmp_gt_u32 vcc, s46, v7\n"
    "v_mfma_f32_16x16x32_bf16 v[28:31], a[16:19], v[144:147], v[28:31]\n"
    "v_exp_f32 v24, v24\n"
    "s_cbranch_vccnz Lfs_60_%=\n"
    "Lfb_59_%=:\n"
    "v_mfma_f32_16x16x32_bf16 v[112:115], a[32:35], v[44:47], v[112:115]\n"
    "v_add_f32 v8, v8, v24\n"
    "s_add_u32 m0, s40, 67584\n"
    "s_xor_b32 s77, s42, 0x2000\n"
    "buffer_load_dwordx4 v3, s[24:27], s42 offen nt lds\n"
    "v_mfma_f32_16x16x32_bf16 v[116:119], a[36:39], v[44:47], v[116:119]\n"
    "v_exp_f32 v25, v25\n"
    "ds_read_b64_tr_b16 a[32:33], v2 offset:25344\n"
    "ds_read_b64_tr_b16 a[34:35], v2 offset:25856\n"
    "v_mfma_f32_16x16x32_bf16 v[120:123], a[40:43], v[44:47], v[120:123]\n"
    "v_add_f32 v8, v8, v25\n"
    "v_cvt_pk_bf16_f32 v40, v24, v25\n"
    "ds_read_b64_tr_b16 a[36:37], v2 offset:25376\n"
    "ds_read_b64_tr_b16 a[38:39], v2 offset:25888\n"
    "v_mfma_f32_16x16x32_bf16 v[124:127], a[44:47], v[44:47], v[124:127]\n"
    "v_exp_f32 v26, v26\n"
    "s_add_u32 m0, m0, 1056\n"
    "s_add_u32 s42, s42, 107520\n"
    "buffer_load_dwordx4 v3, s[24:27], s77 offen nt lds\n"
    "s_and_b32 s42, s42, 0x25cff\n"
    "ds_read_b64_tr_b16 a[40:41], v2 offset:25408\n"
    "ds_read_b64_tr_b16 a[42:43], v2 offset:25920\n"
    "v_mfma_f32_16x16x32_bf16 v[128:131], a[48:51], v[44:47], v[128:131]\n"
    "v_add_f32 v8, v8, v26\n"
    "v_xor_b32 v11, s55, v3\n"
    "v_add_u32 v11, s54, v11\n"
    "v_and_b32 v11, 0xf0, v11\n"
    "ds_read_b64_tr_b16 a[44:45], v2 offset:25440\n"
    "ds_read_b64_tr_b16 a[46:47], v2 offset:25952\n"
    "v_mfma_f32_16x16x32_bf16 v[132:135], a[52:55], v[44:47], v[132:135]\n"
    "v_exp_f32 v27, v27\n"
    "v_lshrrev_b32 v3, 15, v3\n"
    "v_lshl_or_b32 v3, v3, 13, v11\n"
    "ds_read_b64_tr_b16 a[48:49], v2 offset:25472\n"
    "ds_read_b64_tr_b16 a[50:51], v2 offset:25984\n"
    "v_mfma_f32_16x16x32_bf16 v[136:139], a[56:59], v[44:47], v[136:139]\n"
    "v_add_f32 v8, v8, v27\n"
    "v_cvt_pk_bf16_f32 v41, v26, v27\n"
    "s_add_u32 m0, s40, 16896\n"
    "s_nop 0\n"
    "buffer_load_dwordx4 v3, s[28:31], s43 offen lds\n"
    "ds_read_b64_tr_b16 a[52:53], v2 offset:25504\n"
    "ds_read_b64_tr_b16 a[54:55], v2 offset:26016\n"
    "v_mfma_f32_16x16x32_bf16 v[140:143], a[60:63], v[44:47], v[140:143]\n"
    "v_exp_f32 v28, v28\n"
    "ds_read_b64_tr_b16 a[56:57], v2 offset:25536\n"
    "ds_read_b64_tr_b16 a[58:59], v2 offset:26048\n"
    "v_mfma_f32_16x16x32_bf16 v[32:35], a[0:3], v[160:163], v[16:19]\n"
    "v_add_f32 v8, v8, v28\n"
    "ds_read_b64_tr_b16 a[60:61], v2 offset:25568\n"
    "s_waitcnt lgkmcnt(14)\n"
    "ds_read_b64_tr_b16 a[62:63], v2 offset:26080\n"
    "v_mfma_f32_16x16x32_bf16 v[32:35], a[4:7], v[164:167], v[32:35]\n"
    "v_exp_f32 v29, v29\n"
    "s_add_u32 m0, s40, 16928\n"
    "s_nop 0\n"
    "buffer_load_dwordx4 v3, s[28:31], s43 offen offset:1024 lds\n"
    "s_add_u32 s43, s43, 32768\n"
    "v_mfma_f32_16x16x32_bf16 v[32:35], a[8:11], v[168:171], v[32:35]\n"
    "v_add_f32 v8, v8, v29\n"
    "v_cvt_pk_bf16_f32 v42, v28, v29\n"
    "s_add_u32 s66, s65, 8448\n"
    "s_cmp_eq_u32 s66, 42240\n"
    "s_cselect_b32 s66, 0, s66\n"
    "v_mfma_f32_16x16x32_bf16 v[32:35], a[12:15], v[172:175], v[32:35]\n"
    "v_exp_f32 v30, v30\n"
    "v_mfma_f32_16x16x32_bf16 v[36:39], a[28:31], v[172:175], v[16:19]\n"
    "v_add_f32 v8, v8, v30\n"
    "v_mfma_f32_16x16x32_bf16 v[36:39], a[24:27], v[168:171], v[36:39]\n"
    "v_exp_f32 v31, v31\n"
    "v_mfma_f32_16x16x32_bf16 v[36:39], a[20:23], v[164:167], v[36:39]\n"
    "v_add_f32 v8, v8, v31\n"
    "v_cvt_pk_bf16_f32 v43, v30, v31\n"
    "v_mfma_f32_16x16x32_bf16 v[36:39], a[16:19], v[160:163], v[36:39]\n"
    "v_exp_f32 v32, v32\n"
    "s_waitcnt lgkmcnt(0)\n"
    "v_mfma_f32_16x16x32_bf16 v[48:51], a[32:35], v[40:43], v[48:51]\n"
    "v_add_f32 v9, v9, v32\n"
    "v_mfma_f32_16x16x32_bf16 v[52:55], a[36:39], v[40:43], v[52:55]\n"
    "v_exp_f32 v33, v33\n"
    "v_mfma_f32_16x16x32_bf16 v[56:59], a[40:43], v[40:43], v[56:59]\n"
    "v_add_f32 v9, v9, v33\n"
    "v_cvt_pk_bf16_f32 v44, v32, v33\n"
    "v_mfma_f32_16x16x32_bf16 v[60:63], a[44:47], v[40:43], v[60:63]\n"
    "v_exp_f32 v34, v34\n"
    "v_mfma_f32_16x16x32_bf16 v[64:67], a[48:51], v[40:43], v[64:67]\n"
    "v_add_f32 v9, v9, v34\n"
    "v_add_u32 v11, s65, v1\n"
    "v_mfma_f32_16x16x32_bf16 v[68:71], a[52:55], v[40:43], v[68:71]\n"
    "v_exp_f32 v35, v35\n"
    "v_mfma_f32_16x16x32_bf16 v[72:75], a[56:59], v[40:43], v[72:75]\n"
    "v_add_f32 v9, v9, v35\n"
    "v_cvt_pk_bf16_f32 v45, v34, v35\n"
    "v_mfma_f32_16x16x32_bf16 v[76:79], a[60:63], v[40:43], v[76:79]\n"
    "v_exp_f32 v36, v36\n"
    "s_waitcnt vmcnt(8)\n"
    "ds_add_u32 v4, v6\n"
    "s_add_u32 s46, s46, 1\n"
    "v_mfma_f32_16x16x32_bf16 v[24:27], a[0:3], v[176:179], v[20:23]\n"
    "v_add_f32 v9, v9, v36\n"
    "s_memrealtime s[80:81]\n"
    "v_mfma_f32_16x16x32_bf16 v[24:27], a[4:7], v[180:183], v[24:27]\n"
    "v_exp_f32 v37, v37\n"
    "ds_read_b128 a[0:3], v11 offset:0\n"
    "v_mfma_f32_16x16x32_bf16 v[24:27], a[8:11], v[184:187], v[24:27]\n"
    "v_add_f32 v9, v9, v37\n"
    "v_cvt_pk_bf16_f32 v46, v36, v37\n"
    "ds_read_b128 a[4:7], v11 offset:64\n"
    "v_mfma_f32_16x16x32_bf16 v[24:27], a[12:15], v[188:191], v[24:27]\n"
    "v_exp_f32 v38, v38\n"
    "ds_read_b128 a[8:11], v11 offset:128\n"
    "v_mfma_f32_16x16x32_bf16 v[28:31], a[28:31], v[188:191], v[20:23]\n"
    "v_add_f32 v9, v9, v38\n"
    "ds_read_b128 a[12:15], v11 offset:192\n"
    "v_mfma_f32_16x16x32_bf16 v[28:31], a[24:27], v[184:187], v[28:31]\n"
    "v_exp_f32 v39, v39\n"
    "ds_read_b128 a[28:31], v11 offset:704\n"
    "v_mfma_f32_16x16x32_bf16 v[28:31], a[20:23], v[180:183], v[28:31]\n"
    "v_add_f32 v9, v9, v39\n"
    "v_cvt_pk_bf16_f32 v47, v38, v39\n"
    "ds_read_b128 a[24:27], v11 offset:640\n"
    "v_mfma_f32_16x16x32_bf16 v[28:31], a[16:19], v[176:179], v[28:31]\n"
    "v_exp_f32 v24, v24\n"
    "ds_read_b128 a[20:23], v11 offset:576\n"
    "v_mfma_f32_16x16x32_bf16 v[80:83], a[32:35], v[44:47], v[80:83]\n"
    "v_add_f32 v10, v10, v24\n"
    "ds_read_b128 a[16:19], v11 offset:512\n"
    "s_cmp_ge_u32 s46, %[nsw]\n"
    "s_cselect_b32 s83, s79, s83\n"
    "v_mfma_f32_16x16x32_bf16 v[84:87], a[36:39], v[44:47], v[84:87]\n"
    "v_exp_f32 v25, v25\n"
    "v_mfma_f32_16x16x32_bf16 v[88:91], a[40:43], v[44:47], v[88:91]\n"
    "v_add_f32 v10, v10, v25\n"
    "v_cvt_pk_bf16_f32 v40, v24, v25\n"
    "v_mfma_f32_16x16x32_bf16 v[92:95], a[44:47], v[44:47], v[92:95]\n"
    "v_exp_f32 v26, v26\n"
    "v_mfma_f32_16x16x32_bf16 v[96:99], a[48:51], v[44:47], v[96:99]\n"
    "v_add_f32 v10, v10, v26\n"
    "v_mfma_f32_16x16x32_bf16 v[100:103], a[52:55], v[44:47], v[100:103]\n"
    "v_exp_f32 v27, v27\n"
    "v_mfma_f32_16x16x32_bf16 v[104:107], a[56:59], v[44:47], v[104:107]\n"
    "v_add_f32 v10, v10, v27\n"
    "v_cvt_pk_bf16_f32 v41, v26, v27\n"
    "v_mfma_f32_16x16x32_bf16 v[108:111], a[60:63], v[44:47], v[108:111]\n"
    "v_exp_f32 v28, v28\n"
    "s_waitcnt lgkmcnt(0)\n"
    "s_sub_u32 s81, s82, s80\n"
    "s_cmp_gt_i32 s81, 0\n"
    "s_cbranch_scc1 Ltsl_58_%=\n"
    "s_max_i32 s81, s81, 0\n"
    "s_add_u32 s82, s80, s81\n"
    "Ltb_57_%=:\n"
    "s_add_u32 s82, s82, s83\n"
    "v_mfma_f32_16x16x32_bf16 v[32:35], a[0:3], v[144:147], v[12:15]\n"
    "v_add_f32 v10, v10, v28\n"
    "v_mfma_f32_16x16x32_bf16 v[32:35], a[4:7], v[148:151], v[32:35]\n"
    "v_exp_f32 v29, v29\n"
    "ds_read_b32 v7, v5\n"
    "v_mfma_f32_16x16x32_bf16 v[32:35], a[8:11], v[152:155], v[32:35]\n"
    "v_add_f32 v10, v10, v29\n"
    "v_cvt_pk_bf16_f32 v42, v28, v29\n"
    "v_mfma_f32_16x16x32_bf16 v[32:35], a[12:15], v[156:159], v[32:35]\n"
    "v_exp_f32 v30, v30\n"
    "v_mfma_f32_16x16x32_bf16 v[36:39], a[28:31], v[156:159], v[12:15]\n"
    "v_add_f32 v10, v10, v30\n"
    "v_mfma_f32_16x16x32_bf16 v[36:39], a[24:27], v[152:155], v[36:39]\n"
    "v_exp_f32 v31, v31\n"
    "v_mfma_f32_16x16x32_bf16 v[36:39], a[20:23], v[148:151], v[36:39]\n"
    "v_add_f32 v10, v10, v31\n"
    "v_cvt_pk_bf16_f32 v43, v30, v31\n"
    "s_waitcnt lgkmcnt(0)\n"
    "v_cmp_gt_u32 vcc, s46, v7\n"
    "v_mfma_f32_16x16x32_bf16 v[36:39], a[16:19], v[144:147], v[36:39]\n"
    "v_exp_f32 v32, v32\n"
    "s_cbranch_vccnz Lfs_64_%=\n"
    "Lfb_63_%=:\n"
    "v_mfma_f32_16x16x32_bf16 v[112:115], a[32:35], v[40:43], v[112:115]\n"
    "v_add_f32 v8, v8, v32\n"
    "s_add_u32 m0, s40, 25344\n"
    "s_nop 0\n"
    "buffer_load_dwordx4 v3, s[28:31], s43 offen lds\n"
    "v_mfma_f32_16x16x32_bf16 v[116:119], a[36:39], v[40:43], v[116:119]\n"
    "v_exp_f32 v33, v33\n"
    "ds_read_b64_tr_b16 a[32:33], v2 offset:33792\n"
    "ds_read_b64_tr_b16 a[34:35], v2 offset:34304\n"
    "v_mfma_f32_16x16x32_bf16 v[120:123], a[40:43], v[40:43], v[120:123]\n"
    "v_add_f32 v8, v8, v33\n"
    "v_cvt_pk_bf16_f32 v44, v32, v33\n"
    "ds_read_b64_tr_b16 a[36:37], v2 offset:33824\n"
    "ds_read_b64_tr_b16 a[38:39], v2 offset:34336\n"
    "v_mfma_f32_16x16x32_bf16 v[124:127], a[44:47], v[40:43], v[124:127]\n"
    "v_exp_f32 v34, v34\n"
    "s_add_u32 m0, s40, 25376\n"
    "s_nop 0\n"
    "buffer_load_dwordx4 v3, s[28:31], s43 offen offset:1024 lds\n"
    "s_add_u32 s43, s43, 32768\n"
    "ds_read_b64_tr_b16 a[40:41], v2 offset:33856\n"
    "ds_read_b64_tr_b16 a[42:43], v2 offset:34368\n"
    "v_mfma_f32_16x16x32_bf16 v[128:131], a[48:51], v[40:43], v[128:131]\n"
    "v_add_f32 v8, v8, v34\n"
    "ds_read_b64_tr_b16 a[44:45], v2 offset:33888\n"
    "ds_read_b64_tr_b16 a[46:47], v2 offset:34400\n"
    "v_mfma_f32_16x16x32_bf16 v[132:135], a[52:55], v[40:43], v[132:135]\n"
    "v_exp_f32 v35, v35\n"
    "ds_read_b64_tr_b16 a[48:49], v2 offset:33920\n"
    "ds_read_b64_tr_b16 a[50:51], v2 offset:34432\n"
    "v_mfma_f32_16x16x32_bf16 v[136:139], a[56:59], v[40:43], v[136:139]\n"
    "v_add_f32 v8, v8, v35\n"
    "v_cvt_pk_bf16_f32 v45, v34, v35\n"
    "s_add_u32 m0, s40, 42240\n"
    "s_nop 0\n"
    "buffer_load_dwordx4 v3, s[28:31], s43 offen lds\n"
    "ds_read_b64_tr_b16 a[52:53], v2 offset:33952\n"
    "ds_read_b64_tr_b16 a[54:55], v2 offset:34464\n"
    "v_mfma_f32_16x16x32_bf16 v[140:143], a[60:63], v[40:43], v[140:143]\n"
    "v_exp_f32 v36, v36\n"
    "ds_read_b64_tr_b16 a[56:57], v2 offset:33984\n"
    "ds_read_b64_tr_b16 a[58:59], v2 offset:34496\n"
    "v_mfma_f32_16x16x32_bf16 v[24:27], a[0:3], v[160:163], v[16:19]\n"
    "v_add_f32 v8, v8, v36\n"
    "ds_read_b64_tr_b16 a[60:61], v2 offset:34016\n"
    "s_waitcnt lgkmcnt(14)\n"
    "ds_read_b64_tr_b16 a[62:63], v2 offset:34528\n"
    "v_mfma_f32_16x16x32_bf16 v[24:27], a[4:7], v[164:167], v[24:27]\n"
    "v_exp_f32 v37, v37\n"
    "s_add_u32 m0, s40, 42272\n"
    "s_nop 0\n"
    "buffer_load_dwordx4 v3, s[28:31], s43 offen offset:1024 lds\n"
    "s_add_u32 s43, s43, 32768\n"
    "v_mfma_f32_16x16x32_bf16 v[24:27], a[8:11], v[168:171], v[24:27]\n"
    "v_add_f32 v8, v8, v37\n"
    "v_cvt_pk_bf16_f32 v46, v36, v37\n"
    "s_add_u32 s67, s66, 8448\n"
    "s_cmp_eq_u32 s67, 42240\n"
    "s_cselect_b32 s67, 0, s67\n"
    "v_mfma_f32_16x16x32_bf16 v[24:27], a[12:15], v[172:175], v[24:27]\n"
    "v_exp_f32 v38, v38\n"
    "v_mfma_f32_16x16x32_bf16 v[28:31], a[28:31], v[172:175], v[16:19]\n"
    "v_add_f32 v8, v8, v38\n"
    "v_mfma_f32_16x16x32_bf16 v[28:31], a[24:27], v[168:171], v[28:31]\n"
    "v_exp_f32 v39, v39\n"
    "v_mfma_f32_16x16x32_bf16 v[28:31], a[20:23], v[164:167], v[28:31]\n"
    "v_add_f32 v8, v8, v39\n"
    "v_cvt_pk_bf16_f32 v47, v38, v39\n"
    "v_mfma_f32_16x16x32_bf16 v[28:31], a[16:19], v[160:163], v[28:31]\n"
    "v_exp_f32 v24, v24\n"
    "s_waitcnt lgkmcnt(0)\n"
    "v_mfma_f32_16x16x32_bf16 v[48:51], a[32:35], v[44:47], v[48:51]\n"
    "v_add_f32 v9, v9, v24\n"
    "v_mfma_f32_16x16x32_bf16 v[52:55], a[36:39], v[44:47], v[52:55]\n"
    "v_exp_f32 v25, v25\n"
    "v_mfma_f32_16x16x32_bf16 v[56:59], a[40:43], v[44:47], v[56:59]\n"
    "v_add_f32 v9, v9, v25\n"
    "v_cvt_pk_bf16_f32 v40, v24, v25\n"
    "v_mfma_f32_16x16x32_bf16 v[60:63], a[44:47], v[44:47], v[60:63]\n"
    "v_exp_f32 v26, v26\n"
    "v_mfma_f32_16x16x32_bf16 v[64:67], a[48:51], v[44:47], v[64:67]\n"
    "v_add_f32 v9, v9, v26\n"
    "v_add_u32 v11, s66, v1\n"
    "v_mfma_f32_16x16x32_bf16 v[68:71], a[52:55], v[44:47], v[68:71]\n"
    "v_exp_f32 v27, v27\n"
    "v_mfma_f32_16x16x32_bf16 v[72:75], a[56:59], v[44:47], v[72:75]\n"
    "v_add_f32 v9, v9, v27\n"
    "v_cvt_pk_bf16_f32 v41, v26, v27\n"
    "v_mfma_f32_16x16x32_bf16 v[76:79], a[60:63], v[44:47], v[76:79]\n"
    "v_exp_f32 v28, v28\n"
    "s_waitcnt vmcnt(8)\n"
    "ds_add_u32 v4, v6\n"
    "s_add_u32 s46, s46, 1\n"
    "v_mfma_f32_16x16x32_bf16 v[32:35], a[0:3], v[176:179], v[20:23]\n"
    "v_add_f32 v9, v9, v28\n"
    "s_memrealtime s[80:81]\n"
    "v_mfma_f32_16x16x32_bf16 v[32:35], a[4:7], v[180:183], v[32:35]\n"
    "v_exp_f32 v29, v29\n"
    "ds_read_b128 a[0:3], v11 offset:0\n"
    "v_mfma_f32_16x16x32_bf16 v[32:35], a[8:11], v[184:187], v[32:35]\n"
    "v_add_f32 v9, v9, v29\n"
    "v_cvt_pk_bf16_f32 v42, v28, v29\n"
    "ds_read_b128 a[4:7], v11 offset:64\n"
    "v_mfma_f32_16x16x32_bf16 v[32:35], a[12:15], v[188:191], v[32:35]\n"
    "v_exp_f32 v30, v30\n"
    "ds_read_b128 a[8:11], v11 offset:128\n"
    "v_mfma_f32_16x16x32_bf16 v[36:39], a[28:31], v[188:191], v[20:23]\n"
    "v_add_f32 v9, v9, v30\n"
    "ds_read_b128 a[12:15], v11 offset:192\n"
    "v_mfma_f32_16x16x32_bf16 v[36:39], a[24:27], v[184:187], v[36:39]\n"
    "v_exp_f32 v31, v31\n"
    "ds_read_b128 a[28:31], v11 offset:704\n"
    "v_mfma_f32_16x16x32_bf16 v[36:39], a[20:23], v[180:183], v[36:39]\n"
    "v_add_f32 v9, v9, v31\n"
    "v_cvt_pk_bf16_f32 v43, v30, v31\n"
    "ds_read_b128 a[24:27], v11 offset:640\n"
    "v_mfma_f32_16x16x32_bf16 v[36:39], a[16:19], v[176:179], v[36:39]\n"
    "v_exp_f32 v32, v32\n"
    "ds_read_b128 a[20:23], v11 offset:576\n"
    "v_mfma_f32_16x16x32_bf16 v[80:83], a[32:35], v[40:43], v[80:83]\n"
    "v_add_f32 v10, v10, v32\n"
    "ds_read_b128 a[16:19], v11 offset:512\n"
    "s_cmp_ge_u32 s46, %[nsw]\n"
    "s_cselect_b32 s83, s79, s83\n"
    "v_mfma_f32_16x16x32_bf16 v[84:87], a[36:39], v[40:43], v[84:87]\n"
    "v_exp_f32 v33, v33\n"
    "v_mfma_f32_16x16x32_bf16 v[88:91], a[40:43], v[40:43], v[88:91]\n"
    "v_add_f32 v10, v10, v33\n"
    "v_cvt_pk_bf16_f32 v44, v32, v33\n"
    "v_mfma_f32_16x16x32_bf16 v[92:95], a[44:47], v[40:43], v[92:95]\n"
    "v_exp_f32 v34, v34\n"
    "v_mfma_f32_16x16x32_bf16 v[96:99], a[48:51], v[40:43], v[96:99]\n"
    "v_add_f32 v10, v10, v34\n"
    "v_mfma_f32_16x16x32_bf16 v[100:103], a[52:55], v[40:43], v[100:103]\n"
    "v_exp_f32 v35, v35\n"
    "v_mfma_f32_16x16x32_bf16 v[104:107], a[56:59], v[40:43], v[104:107]\n"
    "v_add_f32 v10, v10, v35\n"
    "v_cvt_pk_bf16_f32 v45, v34, v35\n"
    "v_mfma_f32_16x16x32_bf16 v[108:111], a[60:63], v[40:43], v[108:111]\n"
    "v_exp_f32 v36, v36\n"
    "s_waitcnt lgkmcnt(0)\n"
    "s_sub_u32 s81, s82, s80\n"
    "s_cmp_gt_i32 s81, 0\n"
    "s_cbranch_scc1 Ltsl_62_%=\n"
    "s_max_i32 s81, s81, 0\n"
    "s_add_u32 s82, s80, s81\n"
    "Ltb_61_%=:\n"
    "s_add_u32 s82, s82, s83\n"
    "v_mfma_f32_16x16x32_bf16 v[24:27], a[0:3], v[144:147], v[12:15]\n"
    "v_add_f32 v10, v10, v36\n"
    "v_mfma_f32_16x16x32_bf16 v[24:27], a[4:7], v[148:151], v[24:27]\n"
    "v_exp_f32 v37, v37\n"
    "ds_read_b32 v7, v5\n"
    "v_mfma_f32_16x16x32_bf16 v[24:27], a[8:11], v[152:155], v[24:27]\n"
    "v_add_f32 v10, v10, v37\n"
    "v_cvt_pk_bf16_f32 v46, v36, v37\n"
    "v_mfma_f32_16x16x32_bf16 v[24:27], a[12:15], v[156:159], v[24:27]\n"
    "v_exp_f32 v38, v38\n"
    "v_mfma_f32_16x16x32_bf16 v[28:31], a[28:31], v[156:159], v[12:15]\n"
    "v_add_f32 v10, v10, v38\n"
    "v_mfma_f32_16x16x32_bf16 v[28:31], a[24:27], v[152:155], v[28:31]\n"
    "v_exp_f32 v39, v39\n"
    "v_mfma_f32_16x16x32_bf16 v[28:31], a[20:23], v[148:151], v[28:31]\n"
    "v_add_f32 v10, v10, v39\n"
    "v_cvt_pk_bf16_f32 v47, v38, v39\n"
    "s_waitcnt lgkmcnt(0)\n"
    "v_cmp_gt_u32 vcc, s46, v7\n"
    "v_mfma_f32_16x16x32_bf16 v[28:31], a[16:19], v[144:147], v[28:31]\n"
    "v_exp_f32 v24, v24\n"
    "s_cbranch_vccnz Lfs_68_%=\n"
    "Lfb_67_%=:\n"
    "v_mfma_f32_16x16x32_bf16 v[112:115], a[32:35], v[44:47], v[112:115]\n"
    "v_add_f32 v8, v8, v24\n"
    "s_add_u32 m0, s40, 33792\n"
    "s_nop 0\n"
    "buffer_load_dwordx4 v3, s[28:31], s43 offen lds\n"
    "v_mfma_f32_16x16x32_bf16 v[116:119], a[36:39], v[44:47], v[116:119]\n"
    "v_exp_f32 v25, v25\n"
    "ds_read_b64_tr_b16 a[32:33], v2 offset:42240\n"
    "ds_read_b64_tr_b16 a[34:35], v2 offset:42752\n"
    "v_mfma_f32_16x16x32_bf16 v[120:123], a[40:43], v[44:47], v[120:123]\n"
    "v_add_f32 v8, v8, v25\n"
    "v_cvt_pk_bf16_f32 v40, v24, v25\n"
    "ds_read_b64_tr_b16 a[36:37], v2 offset:42272\n"
    "ds_read_b64_tr_b16 a[38:39], v2 offset:42784\n"
    "v_mfma_f32_16x16x32_bf16 v[124:127], a[44:47], v[44:47], v[124:127]\n"
    "v_exp_f32 v26, v26\n"
    "s_add_u32 m0, s40, 33824\n"
    "s_nop 0\n"
    "buffer_load_dwordx4 v3, s[28:31], s43 offen offset:1024 lds\n"
    "s_add_u32 s43, s43, 32768\n"
    "s_bfe_u32 s41, s48, 196612\n"
    "s_lshl_b32 s43, s41, 17\n"
    "ds_read_b64_tr_b16 a[40:41], v2 offset:42304\n"
    "ds_read_b64_tr_b16 a[42:43], v2 offset:42816\n"
    "v_mfma_f32_16x16x32_bf16 v[128:131], a[48:51], v[44:47], v[128:131]\n"
    "v_add_f32 v8, v8, v26\n"
    "ds_read_b64_tr_b16 a[44:45], v2 offset:42336\n"
    "ds_read_b64_tr_b16 a[46:47], v2 offset:42848\n"
    "v_mfma_f32_16x16x32_bf16 v[132:135], a[52:55], v[44:47], v[132:135]\n"
    "v_exp_f32 v27, v27\n"
    "ds_read_b64_tr_b16 a[48:49], v2 offset:42368\n"
    "ds_read_b64_tr_b16 a[50:51], v2 offset:42880\n"
    "v_mfma_f32_16x16x32_bf16 v[136:139], a[56:59], v[44:47], v[136:139]\n"
    "v_add_f32 v8, v8, v27\n"
    "v_cvt_pk_bf16_f32 v41, v26, v27\n"
    "s_add_u32 m0, s40, 50688\n"
    "s_nop 0\n"
    "buffer_load_dwordx4 v3, s[28:31], s43 offen lds\n"
    "ds_read_b64_tr_b16 a[52:53], v2 offset:42400\n"
    "ds_read_b64_tr_b16 a[54:55], v2 offset:42912\n"
    "v_mfma_f32_16x16x32_bf16 v[140:143], a[60:63], v[44:47], v[140:143]\n"
    "v_exp_f32 v28, v28\n"
    "ds_read_b64_tr_b16 a[56:57], v2 offset:42432\n"
    "ds_read_b64_tr_b16 a[58:59], v2 offset:42944\n"
    "v_mfma_f32_16x16x32_bf16 v[32:35], a[0:3], v[160:163], v[16:19]\n"
    "v_add_f32 v8, v8, v28\n"
    "ds_read_b64_tr_b16 a[60:61], v2 offset:42464\n"
    "s_waitcnt lgkmcnt(14)\n"
    "ds_read_b64_tr_b16 a[62:63], v2 offset:42976\n"
    "v_mfma_f32_16x16x32_bf16 v[32:35], a[4:7], v[164:167], v[32:35]\n"
    "v_exp_f32 v29, v29\n"
    "s_add_u32 m0, s40, 50720\n"
    "s_nop 0\n"
    "buffer_load_dwordx4 v3, s[28:31], s43 offen offset:1024 lds\n"
    "s_add_u32 s43, s43, 32768\n"
    "v_mfma_f32_16x16x32_bf16 v[32:35], a[8:11], v[168:171], v[32:35]\n"
    "v_add_f32 v8, v8, v29\n"
    "v_cvt_pk_bf16_f32 v42, v28, v29\n"
    "s_add_u32 s64, s67, 8448\n"
    "s_cmp_eq_u32 s64, 42240\n"
    "s_cselect_b32 s64, 0, s64\n"
    "v_mfma_f32_16x16x32_bf16 v[32:35], a[12:15], v[172:175], v[32:35]\n"
    "v_exp_f32 v30, v30\n"
    "v_mfma_f32_16x16x32_bf16 v[36:39], a[28:31], v[172:175], v[16:19]\n"
    "v_add_f32 v8, v8, v30\n"
    "v_mfma_f32_16x16x32_bf16 v[36:39], a[24:27], v[168:171], v[36:39]\n"
    "v_exp_f32 v31, v31\n"
    "v_mfma_f32_16x16x32_bf16 v[36:39], a[20:23], v[164:167], v[36:39]\n"
    "v_add_f32 v8, v8, v31\n"
    "v_cvt_pk_bf16_f32 v43, v30, v31\n"
    "v_mfma_f32_16x16x32_bf16 v[36:39], a[16:19], v[160:163], v[36:39]\n"
    "v_exp_f32 v32, v32\n"
    "s_waitcnt lgkmcnt(0)\n"
    "v_mfma_f32_16x16x32_bf16 v[48:51], a[32:35], v[40:43], v[48:51]\n"
    "v_add_f32 v9, v9, v32\n"
    "v_mfma_f32_16x16x32_bf16 v[52:55], a[36:39], v[40:43], v[52:55]\n"
    "v_exp_f32 v33, v33\n"
    "v_mfma_f32_16x16x32_bf16 v[56:59], a[40:43], v[40:43], v[56:59]\n"
    "v_add_f32 v9, v9, v33\n"
    "v_cvt_pk_bf16_f32 v44, v32, v33\n"
    "v_mfma_f32_16x16x32_bf16 v[60:63], a[44:47], v[40:43], v[60:63]\n"
    "v_exp_f32 v34, v34\n"
    "v_mfma_f32_16x16x32_bf16 v[64:67], a[48:51], v[40:43], v[64:67]\n"
    "v_add_f32 v9, v9, v34\n"
    "v_add_u32 v11, s67, v1\n"
    "v_mfma_f32_16x16x32_bf16 v[68:71], a[52:55], v[40:43], v[68:71]\n"
    "v_exp_f32 v35, v35\n"
    "v_mfma_f32_16x16x32_bf16 v[72:75], a[56:59], v[40:43], v[72:75]\n"
    "v_add_f32 v9, v9, v35\n"
    "v_cvt_pk_bf16_f32 v45, v34, v35\n"
    "v_mfma_f32_16x16x32_bf16 v[76:79], a[60:63], v[40:43], v[76:79]\n"
    "v_exp_f32 v36, v36\n"
    "s_waitcnt vmcnt(10)\n"
    "ds_add_u32 v4, v6\n"
    "s_add_u32 s46, s46, 1\n"
    "v_mfma_f32_16x16x32_bf16 v[24:27], a[0:3], v[176:179], v[20:23]\n"
    "v_add_f32 v9, v9, v36\n"
    "s_memrealtime s[80:81]\n"
    "v_mfma_f32_16x16x32_bf16 v[24:27], a[4:7], v[180:183], v[24:27]\n"
    "v_exp_f32 v37, v37\n"
    "ds_read_b128 a[0:3], v11 offset:0\n"
    "v_mfma_f32_16x16x32_bf16 v[24:27], a[8:11], v[184:187], v[24:27]\n"
    "v_add_f32 v9, v9, v37\n"
    "v_cvt_pk_bf16_f32 v46, v36, v37\n"
    "ds_read_b128 a[4:7], v11 offset:64\n"
    "v_mfma_f32_16x16x32_bf16 v[24:27], a[12:15], v[188:191], v[24:27]\n"
    "v_exp_f32 v38, v38\n"
    "ds_read_b128 a[8:11], v11 offset:128\n"
    "v_mfma_f32_16x16x32_bf16 v[28:31], a[28:31], v[188:191], v[20:23]\n"
    "v_add_f32 v9, v9, v38\n"
    "ds_read_b128 a[12:15], v11 offset:192\n"
    "v_mfma_f32_16x16x32_bf16 v[28:31], a[24:27], v[184:187], v[28:31]\n"
    "v_exp_f32 v39, v39\n"
    "ds_read_b128 a[28:31], v11 offset:704\n"
    "v_mfma_f32_16x16x32_bf16 v[28:31], a[20:23], v[180:183], v[28:31]\n"
    "v_add_f32 v9, v9, v39\n"
    "v_cvt_pk_bf16_f32 v47, v38, v39\n"
    "ds_read_b128 a[24:27], v11 offset:640\n"
    "v_mfma_f32_16x16x32_bf16 v[28:31], a[16:19], v[176:179], v[28:31]\n"
    "v_exp_f32 v24, v24\n"
    "ds_read_b128 a[20:23], v11 offset:576\n"
    "v_mfma_f32_16x16x32_bf16 v[80:83], a[32:35], v[44:47], v[80:83]\n"
    "v_add_f32 v10, v10, v24\n"
    "ds_read_b128 a[16:19], v11 offset:512\n"
    "s_cmp_ge_u32 s46, %[nsw]\n"
    "s_cselect_b32 s83, s79, s83\n"
    "v_mfma_f32_16x16x32_bf16 v[84:87], a[36:39], v[44:47], v[84:87]\n"
    "v_exp_f32 v25, v25\n"
    "v_mfma_f32_16x16x32_bf16 v[88:91], a[40:43], v[44:47], v[88:91]\n"
    "v_add_f32 v10, v10, v25\n"
    "v_cvt_pk_bf16_f32 v40, v24, v25\n"
    "v_mfma_f32_16x16x32_bf16 v[92:95], a[44:47], v[44:47], v[92:95]\n"
    "v_exp_f32 v26, v26\n"
    "v_mfma_f32_16x16x32_bf16 v[96:99], a[48:51], v[44:47], v[96:99]\n"
    "v_add_f32 v10, v10, v26\n"
    "v_mfma_f32_16x16x32_bf16 v[100:103], a[52:55], v[44:47], v[100:103]\n"
    "v_exp_f32 v27, v27\n"
    "v_mfma_f32_16x16x32_bf16 v[104:107], a[56:59], v[44:47], v[104:107]\n"
    "v_add_f32 v10, v10, v27\n"
    "v_cvt_pk_bf16_f32 v41, v26, v27\n"
    "v_mfma_f32_16x16x32_bf16 v[108:111], a[60:63], v[44:47], v[108:111]\n"
    "v_exp_f32 v28, v28\n"
    "s_waitcnt lgkmcnt(0)\n"
    "s_sub_u32 s81, s82, s80\n"
    "s_cmp_gt_i32 s81, 0\n"
    "s_cbranch_scc1 Ltsl_66_%=\n"
    "s_max_i32 s81, s81, 0\n"
    "s_add_u32 s82, s80, s81\n"
    "Ltb_65_%=:\n"
    "s_add_u32 s82, s82, s83\n"
    "v_mfma_f32_16x16x32_bf16 v[32:35], a[0:3], v[144:147], v[12:15]\n"
    "v_add_f32 v10, v10, v28\n"
    "v_mfma_f32_16x16x32_bf16 v[32:35], a[4:7], v[148:151], v[32:35]\n"
    "v_exp_f32 v29, v29\n"
    "ds_read_b32 v7, v5\n"
    "v_mfma_f32_16x16x32_bf16 v[32:35], a[8:11], v[152:155], v[32:35]\n"
    "v_add_f32 v10, v10, v29\n"
    "v_cvt_pk_bf16_f32 v42, v28, v29\n"
    "v_mfma_f32_16x16x32_bf16 v[32:35], a[12:15], v[156:159], v[32:35]\n"
    "v_exp_f32 v30, v30\n"
    "v_mfma_f32_16x16x32_bf16 v[36:39], a[28:31], v[156:159], v[12:15]\n"
    "v_add_f32 v10, v10, v30\n"
    "v_mfma_f32_16x16x32_bf16 v[36:39], a[24:27], v[152:155], v[36:39]\n"
    "v_exp_f32 v31, v31\n"
    "v_mfma_f32_16x16x32_bf16 v[36:39], a[20:23], v[148:151], v[36:39]\n"
    "v_add_f32 v10, v10, v31\n"
    "v_cvt_pk_bf16_f32 v43, v30, v31\n"
    "s_waitcnt lgkmcnt(0)\n"
    "v_cmp_gt_u32 vcc, s46, v7\n"
    "v_mfma_f32_16x16x32_bf16 v[36:39], a[16:19], v[144:147], v[36:39]\n"
    "v_exp_f32 v32, v32\n"
    "s_cbranch_vccnz Lfs_70_%=\n"
    "Lfb_69_%=:\n"
    "v_mfma_f32_16x16x32_bf16 v[112:115], a[32:35], v[40:43], v[112:115]\n"
    "v_add_f32 v8, v8, v32\n"
    "s_add_u32 m0, s40, 0\n"
    "s_nop 0\n"
    "buffer_load_dwordx4 v3, s[28:31], s43 offen lds\n"
    "v_mfma_f32_16x16x32_bf16 v[116:119], a[36:39], v[40:43], v[116:119]\n"
    "v_exp_f32 v33, v33\n"
    "ds_read_b64_tr_b16 a[32:33], v2 offset:50688\n"
    "ds_read_b64_tr_b16 a[34:35], v2 offset:51200\n"
    "s_mov_b32 s50, 0\n"
    "v_mfma_f32_16x16x32_bf16 v[120:123], a[40:43], v[40:43], v[120:123]\n"
    "v_add_f32 v8, v8, v33\n"
    "v_cvt_pk_bf16_f32 v44, v32, v33\n"
    "ds_read_b64_tr_b16 a[36:37], v2 offset:50720\n"
    "ds_read_b64_tr_b16 a[38:39], v2 offset:51232\n"
    "v_mfma_f32_16x16x32_bf16 v[124:127], a[44:47], v[40:43], v[124:127]\n"
    "v_exp_f32 v34, v34\n"
    "s_add_u32 m0, s40, 32\n"
    "s_nop 0\n"
    "buffer_load_dwordx4 v3, s[28:31], s43 offen offset:1024 lds\n"
    "s_add_u32 s43, s43, 32768\n"
    "ds_read_b64_tr_b16 a[40:41], v2 offset:50752\n"
    "ds_read_b64_tr_b16 a[42:43], v2 offset:51264\n"
    "v_mfma_f32_16x16x32_bf16 v[128:131], a[48:51], v[40:43], v[128:131]\n"
    "v_add_f32 v8, v8, v34\n"
    "ds_read_b64_tr_b16 a[44:45], v2 offset:50784\n"
    "ds_read_b64_tr_b16 a[46:47], v2 offset:51296\n"
    "v_mfma_f32_16x16x32_bf16 v[132:135], a[52:55], v[40:43], v[132:135]\n"
    "v_exp_f32 v35, v35\n"
    "ds_read_b64_tr_b16 a[48:49], v2 offset:50816\n"
    "ds_read_b64_tr_b16 a[50:51], v2 offset:51328\n"
    "v_mfma_f32_16x16x32_bf16 v[136:139], a[56:59], v[40:43], v[136:139]\n"
    "v_add_f32 v8, v8, v35\n"
    "v_cvt_pk_bf16_f32 v45, v34, v35\n"
    "s_add_u32 m0, s40, 59136\n"
    "s_nop 0\n"
    "buffer_load_dwordx4 v3, s[28:31], s43 offen lds\n"
    "ds_read_b64_tr_b16 a[52:53], v2 offset:50848\n"
    "ds_read_b64_tr_b16 a[54:55], v2 offset:51360\n"
    "v_mfma_f32_16x16x32_bf16 v[140:143], a[60:63], v[40:43], v[140:143]\n"
    "v_exp_f32 v36, v36\n"
    "ds_read_b64_tr_b16 a[56:57], v2 offset:50880\n"
    "ds_read_b64_tr_b16 a[58:59], v2 offset:51392\n"
    "v_mfma_f32_16x16x32_bf16 v[24:27], a[0:3], v[160:163], v[16:19]\n"
    "v_add_f32 v8, v8, v36\n"
    "ds_read_b64_tr_b16 a[60:61], v2 offset:50912\n"
    "s_waitcnt lgkmcnt(14)\n"
    "ds_read_b64_tr_b16 a[62:63], v2 offset:51424\n"
    "v_mfma_f32_16x16x32_bf16 v[24:27], a[4:7], v[164:167], v[24:27]\n"
    "v_exp_f32 v37, v37\n"
    "s_add_u32 m0, s40, 59168\n"
    "s_nop 0\n"
    "buffer_load_dwordx4 v3, s[28:31], s43 offen offset:1024 lds\n"
    "s_add_u32 s43, s43, 32768\n"
    "v_mfma_f32_16x16x32_bf16 v[24:27], a[8:11], v[168:171], v[24:27]\n"
    "v_add_f32 v8, v8, v37\n"
    "v_cvt_pk_bf16_f32 v46, v36, v37\n"
    "v_mfma_f32_16x16x32_bf16 v[24:27], a[12:15], v[172:175], v[24:27]\n"
    "v_exp_f32 v38, v38\n"
    "v_mfma_f32_16x16x32_bf16 v[28:31], a[28:31], v[172:175], v[16:19]\n"
    "v_add_f32 v8, v8, v38\n"
    "v_mfma_f32_16x16x32_bf16 v[28:31], a[24:27], v[168:171], v[28:31]\n"
    "v_exp_f32 v39, v39\n"
    "v_mfma_f32_16x16x32_bf16 v[28:31], a[20:23], v[164:167], v[28:31]\n"
    "v_add_f32 v8, v8, v39\n"
    "v_cvt_pk_bf16_f32 v47, v38, v39\n"
    "v_mov_b32 v19, v3\n"
    "v_mbcnt_lo_u32_b32 v3, -1, 0\n"
    "v_mfma_f32_16x16x32_bf16 v[28:31], a[16:19], v[160:163], v[28:31]\n"
    "v_exp_f32 v24, v24\n"
    "s_waitcnt lgkmcnt(0)\n"
    "v_mbcnt_hi_u32_b32 v3, -1, v3\n"
    "v_lshlrev_b32 v3, 4, v3\n"
    "v_mfma_f32_16x16x32_bf16 v[48:51], a[32:35], v[44:47], v[48:51]\n"
    "v_add_f32 v9, v9, v24\n"
    "v_mov_b32 v11, v8\n"
    "s_nop 1\n"
    "v_mfma_f32_16x16x32_bf16 v[52:55], a[36:39], v[44:47], v[52:55]\n"
    "v_exp_f32 v25, v25\n"
    "v_permlane32_swap_b32 v8, v11\n"
    "s_nop 1\n"
    "v_mfma_f32_16x16x32_bf16 v[56:59], a[40:43], v[44:47], v[56:59]\n"
    "v_add_f32 v9, v9, v25\n"
    "v_cvt_pk_bf16_f32 v40, v24, v25\n"
    "v_add_f32 v8, v8, v11\n"
    "v_mov_b32 v11, v8\n"
    "v_mfma_f32_16x16x32_bf16 v[60:63], a[44:47], v[44:47], v[60:63]\n"
    "v_exp_f32 v26, v26\n"
    "s_nop 1\n"
    "v_permlane16_swap_b32 v8, v11\n"
    "v_mfma_f32_16x16x32_bf16 v[64:67], a[48:51], v[44:47], v[64:67]\n"
    "v_add_f32 v9, v9, v26\n"
    "s_nop 1\n"
    "v_add_f32 v8, v8, v11\n"
    "v_mfma_f32_16x16x32_bf16 v[68:71], a[52:55], v[44:47], v[68:71]\n"
    "v_exp_f32 v27, v27\n"
    "v_cmp_nge_f32 vcc, s58, v8\n"
    "s_or_b32 s50, s50, vcc_lo\n"
    "v_mfma_f32_16x16x32_bf16 v[72:75], a[56:59], v[44:47], v[72:75]\n"
    "v_add_f32 v9, v9, v27\n"
    "v_cvt_pk_bf16_f32 v41, v26, v27\n"
    "s_or_b32 s50, s50, vcc_hi\n"
    "v_rcp_f32 v8, v8\n"
    "v_mfma_f32_16x16x32_bf16 v[76:79], a[60:63], v[44:47], v[76:79]\n"
    "v_exp_f32 v28, v28\n"
    "v_mfma_f32_16x16x32_bf16 v[32:35], a[0:3], v[176:179], v[20:23]\n"
    "v_add_f32 v9, v9, v28\n"
    "v_mfma_f32_16x16x32_bf16 v[32:35], a[4:7], v[180:183], v[32:35]\n"
    "v_exp_f32 v29, v29\n"
    "v_mul_f32 v48, v8, v48\n"
    "v_mul_f32 v49, v8, v49\n"
    "v_mul_f32 v50, v8, v50\n"
    "v_mul_f32 v51, v8, v51\n"
    "v_mfma_f32_16x16x32_bf16 v[32:35], a[8:11], v[184:187], v[32:35]\n"
    "v_add_f32 v9, v9, v29\n"
    "v_cvt_pk_bf16_f32 v42, v28, v29\n"
    "v_mul_f32 v52, v8, v52\n"
    "v_mul_f32 v53, v8, v53\n"
    "v_mul_f32 v54, v8, v54\n"
    "v_mul_f32 v55, v8, v55\n"
    "v_mfma_f32_16x16x32_bf16 v[32:35], a[12:15], v[188:191], v[32:35]\n"
    "v_exp_f32 v30, v30\n"
    "v_cvt_pk_bf16_f32 v48, v48, v49\n"
    "v_cvt_pk_bf16_f32 v49, v50, v51\n"
    "v_cvt_pk_bf16_f32 v50, v52, v53\n"
    "v_cvt_pk_bf16_f32 v51, v54, v55\n"
    "v_mfma_f32_16x16x32_bf16 v[36:39], a[28:31], v[188:191], v[20:23]\n"
    "v_add_f32 v9, v9, v30\n"
    "buffer_store_dwordx4 v[48:51], v3, s[36:39], s60 offen offset:0 sc1\n"
    "v_mul_f32 v56, v8, v56\n"
    "v_mul_f32 v57, v8, v57\n"
    "v_mul_f32 v58, v8, v58\n"
    "v_mfma_f32_16x16x32_bf16 v[36:39], a[24:27], v[184:187], v[36:39]\n"
    "v_exp_f32 v31, v31\n"
    "v_mul_f32 v59, v8, v59\n"
    "v_mul_f32 v60, v8, v60\n"
    "v_mul_f32 v61, v8, v61\n"
    "v_mul_f32 v62, v8, v62\n"
    "v_mfma_f32_16x16x32_bf16 v[36:39], a[20:23], v[180:183], v[36:39]\n"
    "v_add_f32 v9, v9, v31\n"
    "v_cvt_pk_bf16_f32 v43, v30, v31\n"
    "v_mul_f32 v63, v8, v63\n"
    "v_cvt_pk_bf16_f32 v56, v56, v57\n"
    "v_cvt_pk_bf16_f32 v57, v58, v59\n"
    "v_cvt_pk_bf16_f32 v58, v60, v61\n"
    "v_mfma_f32_16x16x32_bf16 v[36:39], a[16:19], v[176:179], v[36:39]\n"
    "v_exp_f32 v32, v32\n"
    "v_cvt_pk_bf16_f32 v59, v62, v63\n"
    "buffer_store_dwordx4 v[56:59], v3, s[36:39], s60 offen offset:1024 sc1\n"
    "v_mul_f32 v64, v8, v64\n"
    "v_mul_f32 v65, v8, v65\n"
    "v_mfma_f32_16x16x32_bf16 v[80:83], a[32:35], v[40:43], v[80:83]\n"
    "v_add_f32 v10, v10, v32\n"
    "v_mov_b32 v11, v9\n"
    "s_nop 1\n"
    "v_mul_f32 v66, v8, v66\n"
    "v_mul_f32 v67, v8, v67\n"
    "v_mul_f32 v68, v8, v68\n"
    "v_mul_f32 v69, v8, v69\n"
    "v_mfma_f32_16x16x32_bf16 v[84:87], a[36:39], v[40:43], v[84:87]\n"
    "v_exp_f32 v33, v33\n"
    "v_permlane32_swap_b32 v9, v11\n"
    "s_nop 1\n"
    "v_mul_f32 v70, v8, v70\n"
    "v_mul_f32 v71, v8, v71\n"
    "v_cvt_pk_bf16_f32 v64, v64, v65\n"
    "v_cvt_pk_bf16_f32 v65, v66, v67\n"
    "v_mfma_f32_16x16x32_bf16 v[88:91], a[40:43], v[40:43], v[88:91]\n"
    "v_add_f32 v10, v10, v33\n"
    "v_cvt_pk_bf16_f32 v44, v32, v33\n"
    "v_add_f32 v9, v9, v11\n"
    "v_mov_b32 v11, v9\n"
    "v_cvt_pk_bf16_f32 v66, v68, v69\n"
    "v_cvt_pk_bf16_f32 v67, v70, v71\n"
    "buffer_store_dwordx4 v[64:67], v3, s[36:39], s60 offen offset:2048 sc1\n"
    "v_mul_f32 v72, v8, v72\n"
    "v_mfma_f32_16x16x32_bf16 v[92:95], a[44:47], v[40:43], v[92:95]\n"
    "v_exp_f32 v34, v34\n"
    "s_nop 1\n"
    "v_permlane16_swap_b32 v9, v11\n"
    "v_mul_f32 v73, v8, v73\n"
    "v_mul_f32 v74, v8, v74\n"
    "v_mul_f32 v75, v8, v75\n"
    "v_mul_f32 v76, v8, v76\n"
    "v_mfma_f32_16x16x32_bf16 v[96:99], a[48:51], v[40:43], v[96:99]\n"
    "v_add_f32 v10, v10, v34\n"
    "s_nop 1\n"
    "v_add_f32 v9, v9, v11\n"
    "v_mul_f32 v77, v8, v77\n"
    "v_mul_f32 v78, v8, v78\n"
    "v_mul_f32 v79, v8, v79\n"
    "v_cvt_pk_bf16_f32 v72, v72, v73\n"
    "v_mfma_f32_16x16x32_bf16 v[100:103], a[52:55], v[40:43], v[100:103]\n"
    "v_exp_f32 v35, v35\n"
    "v_cmp_nge_f32 vcc, s58, v9\n"
    "s_or_b32 s50, s50, vcc_lo\n"
    "v_cvt_pk_bf16_f32 v73, v74, v75\n"
    "v_cvt_pk_bf16_f32 v74, v76, v77\n"
    "v_cvt_pk_bf16_f32 v75, v78, v79\n"
    "buffer_store_dwordx4 v[72:75], v3, s[36:39], s60 offen offset:3072 sc1\n"
    "v_mfma_f32_16x16x32_bf16 v[104:107], a[56:59], v[40:43], v[104:107]\n"
    "v_add_f32 v10, v10, v35\n"
    "v_cvt_pk_bf16_f32 v45, v34, v35\n"
    "s_or_b32 s50, s50, vcc_hi\n"
    "v_rcp_f32 v20, v9\n"
    "v_mfma_f32_16x16x32_bf16 v[108:111], a[60:63], v[40:43], v[108:111]\n"
    "v_exp_f32 v36, v36\n"
    "s_waitcnt lgkmcnt(0)\n"
    "v_rcp_f32 v21, v9\n"
    "v_mov_b32 v22, v8\n"
    "v_mov_b32 v23, v8\n"
    "s_lshl_b32 s54, %[team], 2\n"
    "s_add_u32 s54, s54, %[w]\n"
    "s_lshl_b32 s54, s54, 8\n"
    "s_add_u32 s54, s54, %[lds]\n"
    "s_add_u32 s54, s54, 156160\n"
    "v_min_u32 v16, 0xf0, v3\n"
    "v_add_u32 v16, s54, v16\n"
    "ds_read_b128 v[12:15], v16\n"
    "s_waitcnt lgkmcnt(0)\n"
    "global_load_dword v16, v12, %[ws] sc1\n"
    "v_add_f32 v10, v10, v36\n"
    "v_mul_f32 v80, v20, v80\n"
    "v_mul_f32 v81, v20, v81\n"
    "v_exp_f32 v37, v37\n"
    "v_mul_f32 v82, v20, v82\n"
    "v_mul_f32 v83, v20, v83\n"
    "v_add_f32 v10, v10, v37\n"
    "v_cvt_pk_bf16_f32 v46, v36, v37\n"
    "v_mul_f32 v84, v20, v84\n"
    "v_mul_f32 v85, v20, v85\n"
    "v_exp_f32 v38, v38\n"
    "v_mul_f32 v86, v20, v86\n"
    "v_mul_f32 v87, v20, v87\n"
    "v_add_f32 v10, v10, v38\n"
    "v_cvt_pk_bf16_f32 v80, v80, v81\n"
    "v_cvt_pk_bf16_f32 v81, v82, v83\n"
    "v_exp_f32 v39, v39\n"
    "v_cvt_pk_bf16_f32 v82, v84, v85\n"
    "v_cvt_pk_bf16_f32 v83, v86, v87\n"
    "v_add_f32 v10, v10, v39\n"
    "v_cvt_pk_bf16_f32 v47, v38, v39\n"
    "buffer_store_dwordx4 v[80:83], v3, s[36:39], s61 offen offset:0 sc1\n"
    "v_mul_f32 v88, v20, v88\n"
    "v_mul_f32 v89, v20, v89\n"
    "v_mul_f32 v90, v20, v90\n"
    "v_mul_f32 v91, v20, v91\n"
    "v_mul_f32 v92, v20, v92\n"
    "v_mfma_f32_16x16x32_bf16 v[112:115], a[32:35], v[44:47], v[112:115]\n"
    "v_mov_b32 v11, v10\n"
    "s_nop 1\n"
    "v_mul_f32 v93, v20, v93\n"
    "v_mul_f32 v94, v20, v94\n"
    "v_mul_f32 v95, v20, v95\n"
    "v_mfma_f32_16x16x32_bf16 v[116:119], a[36:39], v[44:47], v[116:119]\n"
    "v_permlane32_swap_b32 v10, v11\n"
    "s_nop 1\n"
    "v_cvt_pk_bf16_f32 v88, v88, v89\n"
    "v_cvt_pk_bf16_f32 v89, v90, v91\n"
    "v_cvt_pk_bf16_f32 v90, v92, v93\n"
    "v_mfma_f32_16x16x32_bf16 v[120:123], a[40:43], v[44:47], v[120:123]\n"
    "v_add_f32 v10, v10, v11\n"
    "v_mov_b32 v11, v10\n"
    "v_cvt_pk_bf16_f32 v91, v94, v95\n"
    "buffer_store_dwordx4 v[88:91], v3, s[36:39], s61 offen offset:1024 sc1\n"
    "v_mul_f32 v96, v20, v96\n"
    "v_mfma_f32_16x16x32_bf16 v[124:127], a[44:47], v[44:47], v[124:127]\n"
    "s_nop 1\n"
    "v_permlane16_swap_b32 v10, v11\n"
    "v_mul_f32 v97, v20, v97\n"
    "v_mul_f32 v98, v20, v98\n"
    "v_mul_f32 v99, v20, v99\n"
    "v_mfma_f32_16x16x32_bf16 v[128:131], a[48:51], v[44:47], v[128:131]\n"
    "s_nop 1\n"
    "v_add_f32 v10, v10, v11\n"
    "v_mul_f32 v100, v20, v100\n"
    "v_mul_f32 v101, v20, v101\n"
    "v_mul_f32 v102, v20, v102\n"
    "v_mfma_f32_16x16x32_bf16 v[132:135], a[52:55], v[44:47], v[132:135]\n"
    "v_cmp_nge_f32 vcc, s58, v10\n"
    "s_or_b32 s50, s50, vcc_lo\n"
    "v_mul_f32 v103, v20, v103\n"
    "v_cvt_pk_bf16_f32 v96, v96, v97\n"
    "v_cvt_pk_bf16_f32 v97, v98, v99\n"
    "v_mfma_f32_16x16x32_bf16 v[136:139], a[56:59], v[44:47], v[136:139]\n"
    "s_or_b32 s50, s50, vcc_hi\n"
    "v_rcp_f32 v11, v10\n"
    "v_cvt_pk_bf16_f32 v98, v100, v101\n"
    "v_cvt_pk_bf16_f32 v99, v102, v103\n"
    "buffer_store_dwordx4 v[96:99], v3, s[36:39], s61 offen offset:2048 sc1\n"
    "v_mfma_f32_16x16x32_bf16 v[140:143], a[60:63], v[44:47], v[140:143]\n"
    "v_rcp_f32 v10, v10\n"
    "v_mul_f32 v104, v20, v104\n"
    "v_mul_f32 v105, v20, v105\n"
    "v_mul_f32 v106, v20, v106\n"
    "v_mov_b32 v7, s50\n"
    "ds_write_b32 v4, v7 offset:2048\n"
    "s_waitcnt vmcnt(18)\n"
    "ds_add_u32 v4, v6\n"
    "s_add_u32 s46, s46, 1\n"
    "v_mul_f32 v107, v20, v107\n"
    "v_mul_f32 v108, v20, v108\n"
    "v_mul_f32 v109, v20, v109\n"
    "v_mul_f32 v110, v20, v110\n"
    "v_mul_f32 v111, v20, v111\n"
    "v_cvt_pk_bf16_f32 v104, v104, v105\n"
    "v_cvt_pk_bf16_f32 v105, v106, v107\n"
    "v_cvt_pk_bf16_f32 v106, v108, v109\n"
    "v_cvt_pk_bf16_f32 v107, v110, v111\n"
    "buffer_store_dwordx4 v[104:107], v3, s[36:39], s61 offen offset:3072 sc1\n"
    "v_mul_f32 v112, v10, v112\n"
    "v_mul_f32 v113, v10, v113\n"
    "v_mul_f32 v114, v10, v114\n"
    "v_mul_f32 v115, v10, v115\n"
    "v_mul_f32 v116, v10, v116\n"
    "v_mul_f32 v117, v10, v117\n"
    "v_mul_f32 v118, v10, v118\n"
    "v_mul_f32 v119, v10, v119\n"
    "v_cvt_pk_bf16_f32 v112, v112, v113\n"
    "v_cvt_pk_bf16_f32 v113, v114, v115\n"
    "v_cvt_pk_bf16_f32 v114, v116, v117\n"
    "v_cvt_pk_bf16_f32 v115, v118, v119\n"
    "buffer_store_dwordx4 v[112:115], v3, s[36:39], s62 offen offset:0 sc1\n"
    "v_mul_f32 v120, v10, v120\n"
    "v_mul_f32 v121, v10, v121\n"
    "v_mul_f32 v122, v10, v122\n"
    "v_mul_f32 v123, v10, v123\n"
    "v_mul_f32 v124, v10, v124\n"
    "v_mul_f32 v125, v10, v125\n"
    "v_mul_f32 v126, v10, v126\n"
    "v_mul_f32 v127, v10, v127\n"
    "v_cvt_pk_bf16_f32 v120, v120, v121\n"
    "v_cvt_pk_bf16_f32 v121, v122, v123\n"
    "v_cvt_pk_bf16_f32 v122, v124, v125\n"
    "v_cvt_pk_bf16_f32 v123, v126, v127\n"
    "buffer_store_dwordx4 v[120:123], v3, s[36:39], s62 offen offset:1024 sc1\n"
    "v_mul_f32 v128, v10, v128\n"
    "v_mul_f32 v129, v10, v129\n"
    "v_mul_f32 v130, v10, v130\n"
    "v_mul_f32 v131, v10, v131\n"
    "v_mul_f32 v132, v10, v132\n"
    "v_mul_f32 v133, v10, v133\n"
    "v_mul_f32 v134, v10, v134\n"
    "v_mul_f32 v135, v10, v135\n"
    "v_cvt_pk_bf16_f32 v128, v128, v129\n"
    "v_cvt_pk_bf16_f32 v129, v130, v131\n"
    "v_cvt_pk_bf16_f32 v130, v132, v133\n"
    "v_cvt_pk_bf16_f32 v131, v134, v135\n"
    "buffer_store_dwordx4 v[128:131], v3, s[36:39], s62 offen offset:2048 sc1\n"
    "v_mul_f32 v136, v10, v136\n"
    "v_mul_f32 v137, v10, v137\n"
    "v_mul_f32 v138, v10, v138\n"
    "v_mul_f32 v139, v10, v139\n"
    "v_mul_f32 v140, v10, v140\n"
    "v_mul_f32 v141, v10, v141\n"
    "v_mul_f32 v142, v10, v142\n"
    "v_mul_f32 v143, v10, v143\n"
    "v_cvt_pk_bf16_f32 v136, v136, v137\n"
    "v_cvt_pk_bf16_f32 v137, v138, v139\n"
    "v_cvt_pk_bf16_f32 v138, v140, v141\n"
    "v_cvt_pk_bf16_f32 v139, v142, v143\n"
    "buffer_store_dwordx4 v[136:139], v3, s[36:39], s62 offen offset:3072 sc1\n"
    "s_call_b64 s[52:53], Lspin_%=\n"
    "ds_read_b32 v7, v5 offset:2048\n"
    "s_xor_b32 s54, %[team], 0\n"
    "s_lshl_b32 s54, s54, 2\n"
    "s_add_u32 s54, s54, %[w]\n"
    "s_lshl_b32 s54, s54, 2\n"
    "s_add_u32 s54, s54, %[lds]\n"
    "s_add_u32 s54, s54, 158208\n"
    "v_mov_b32 v20, s54\n"
    "s_xor_b32 s54, %[team], 1\n"
    "s_lshl_b32 s54, s54, 2\n"
    "s_add_u32 s54, s54, %[w]\n"
    "s_lshl_b32 s54, s54, 2\n"
    "s_add_u32 s54, s54, %[lds]\n"
    "s_add_u32 s54, s54, 158208\n"
    "v_mov_b32 v21, s54\n"
    "s_waitcnt lgkmcnt(0)\n"
    "v_cmp_ne_u32 vcc, 0, v7\n"
    "s_cbranch_vccnz Lslow_%=\n"
    "Ldone_%=:\n"
    "s_add_u32 m0, s40, 8448\n"
    "s_nop 0\n"
    "buffer_load_dwordx4 v19, s[28:31], s43 offen lds\n"
    "s_add_u32 m0, s40, 8480\n"
    "s_nop 0\n"
    "buffer_load_dwordx4 v19, s[28:31], s43 offen offset:1024 lds\n"
    "s_add_u32 s43, s43, 32768\n"
    "v_subrev_u32 v2, 16896, v2\n"
    "ds_read_b64_tr_b16 a[0:1], v2 offset:16896\n"
    "ds_read_b64_tr_b16 a[2:3], v2 offset:17408\n"
    "ds_read_b64_tr_b16 a[4:5], v2 offset:16928\n"
    "ds_read_b64_tr_b16 a[6:7], v2 offset:17440\n"
    "ds_read_b64_tr_b16 a[8:9], v2 offset:16960\n"
    "ds_read_b64_tr_b16 a[10:11], v2 offset:17472\n"
    "ds_read_b64_tr_b16 a[12:13], v2 offset:16992\n"
    "ds_read_b64_tr_b16 a[14:15], v2 offset:17504\n"
    "ds_read_b64_tr_b16 a[16:17], v2 offset:17024\n"
    "ds_read_b64_tr_b16 a[18:19], v2 offset:17536\n"
    "ds_read_b64_tr_b16 a[20:21], v2 offset:17056\n"
    "ds_read_b64_tr_b16 a[22:23], v2 offset:17568\n"
    "ds_read_b64_tr_b16 a[24:25], v2 offset:17088\n"
    "ds_read_b64_tr_b16 a[26:27], v2 offset:17600\n"
    "v_mov_b64 v[144:145], v[48:49]\n"
    "v_mov_b64 v[146:147], v[50:51]\n"
    "v_mov_b64 v[148:149], v[80:81]\n"
    "v_mov_b64 v[150:151], v[82:83]\n"
    "v_mov_b64 v[152:153], v[112:113]\n"
    "v_mov_b64 v[154:155], v[114:115]\n"
    "v_mov_b64 v[156:157], v[56:57]\n"
    "v_mov_b64 v[158:159], v[58:59]\n"
    "v_mov_b64 v[160:161], v[88:89]\n"
    "v_mov_b64 v[162:163], v[90:91]\n"
    "v_mov_b64 v[164:165], v[120:121]\n"
    "v_mov_b64 v[166:167], v[122:123]\n"
    "v_mov_b64 v[168:169], v[64:65]\n"
    "v_mov_b64 v[170:171], v[66:67]\n"
    "v_mov_b64 v[172:173], v[96:97]\n"
    "v_mov_b64 v[174:175], v[98:99]\n"
    "v_mov_b64 v[176:177], v[128:129]\n"
    "v_mov_b64 v[178:179], v[130:131]\n"
    "v_mov_b64 v[180:181], v[72:73]\n"
    "v_mov_b64 v[182:183], v[74:75]\n"
    "v_mov_b64 v[184:185], v[104:105]\n"
    "v_mov_b64 v[186:187], v[106:107]\n"
    "v_mov_b64 v[188:189], v[136:137]\n"
    "v_mov_b64 v[190:191], v[138:139]\n"
    "v_mov_b64 v[48:49], 0\n"
    "v_mov_b64 v[50:51], 0\n"
    "v_mov_b64 v[52:53], 0\n"
    "v_mov_b64 v[54:55], 0\n"
    "v_mov_b64 v[56:57], 0\n"
    "v_mov_b64 v[58:59], 0\n"
    "v_mov_b64 v[60:61], 0\n"
    "v_mov_b64 v[62:63], 0\n"
    "v_mov_b64 v[64:65], 0\n"
    "v_mov_b64 v[66:67], 0\n"
    "v_mov_b64 v[68:69], 0\n"
    "v_mov_b64 v[70:71], 0\n"
    "v_mov_b64 v[72:73], 0\n"
    "v_mov_b64 v[74:75], 0\n"
    "v_mov_b64 v[76:77], 0\n"
    "v_mov_b64 v[78:79], 0\n"
    "v_mov_b64 v[80:81], 0\n"
    "v_mov_b64 v[82:83], 0\n"
    "v_mov_b64 v[84:85], 0\n"
    "v_mov_b64 v[86:87], 0\n"
    "v_mov_b64 v[88:89], 0\n"
    "v_mov_b64 v[90:91], 0\n"
    "v_mov_b64 v[92:93], 0\n"
    "v_mov_b64 v[94:95], 0\n"
    "v_mov_b64 v[96:97], 0\n"
    "v_mov_b64 v[98:99], 0\n"
    "v_mov_b64 v[100:101], 0\n"
    "v_mov_b64 v[102:103], 0\n"
    "v_mov_b64 v[104:105], 0\n"
    "v_mov_b64 v[106:107], 0\n"
    "v_mov_b64 v[108:109], 0\n"
    "v_mov_b64 v[110:111], 0\n"
    "v_mov_b64 v[112:113], 0\n"
    "v_mov_b64 v[114:115], 0\n"
    "v_mov_b64 v[116:117], 0\n"
    "v_mov_b64 v[118:119], 0\n"
    "v_mov_b64 v[120:121], 0\n"
    "v_mov_b64 v[122:123], 0\n"
    "v_mov_b64 v[124:125], 0\n"
    "v_mov_b64 v[126:127], 0\n"
    "v_mov_b64 v[128:129], 0\n"
    "v_mov_b64 v[130:131], 0\n"
    "v_mov_b64 v[132:133], 0\n"
    "v_mov_b64 v[134:135], 0\n"
    "v_mov_b64 v[136:137], 0\n"
    "v_mov_b64 v[138:139], 0\n"
    "v_mov_b64 v[140:141], 0\n"
    "v_mov_b64 v[142:143], 0\n"
    "s_mul_i32 s54, %[team], 12\n"
    "s_sub_u32 s84, 0x77f, s54\n"
    "s_lshl_b32 s54, %[team], 1\n"
    "s_sub_u32 s42, 5, s54\n"
    "s_lshr_b32 s78, 16, s54\n"
    "s_sub_u32 s77, 3, s54\n"
    "s_mov_b32 s44, 0\n"
    "s_mov_b32 s45, 1\n"
    "s_setprio 0\n"
    "s_cmp_lg_u32 %[team], 0\n"
    "s_cbranch_scc1 Lop_%=\n"
    "s_setprio 0\n"
    "Lop_%=:\n"
    "s_waitcnt vmcnt(2)\n"
    "s_mov_b64 exec, 0x700\n"
    "v_bfi_b32 v18, v15, v14, v16\n"
    "global_store_dword v13, v18, %[ws] sc1\n"
    "s_mov_b64 exec, -1\n"
    "s_mov_b64 s[36:37], %[ws]\n"
    "s_add_u32 s36, s36, %[op2]\n"
    "s_addc_u32 s37, s37, 0\n"
    "s_and_b32 s37, s37, 0xffff\n"
    "s_mov_b32 s38, 356352\n"
    "s_mov_b32 s39, 0x20000\n"
    "s_bfe_u32 s41, s48, 196612\n"
    "s_mul_i32 s60, s41, 49152\n"
    "s_add_u32 s61, s60, 4096\n"
    "s_add_u32 s62, s60, 8192\n"
    "Ltop0_%=:\n"
    "s_cmp_eq_u32 s45, 0\n"
    "s_cbranch_scc1 Lst0_%=\n"
    "Lgo0_%=:\n"
    "s_or_b32 s54, s84, s78\n"
    "s_bitcmp0_b32 s54, s44\n"
    "s_cselect_b32 s76, 1, 0\n"
    "s_cselect_b32 s45, 1, 0\n"
    "Lgp0_%=:\n"
    "s_waitcnt lgkmcnt(12)\n"
    "v_mfma_f32_16x16x32_bf16 v[48:51], a[0:3], v[144:147], v[48:51]\n"
    "ds_read_b64_tr_b16 a[28:29], v2 offset:17120\n"
    "v_mfma_f32_16x16x32_bf16 v[80:83], a[0:3], v[148:151], v[80:83]\n"
    "ds_read_b64_tr_b16 a[30:31], v2 offset:17632\n"
    "v_mfma_f32_16x16x32_bf16 v[112:115], a[0:3], v[152:155], v[112:115]\n"
    "s_cmp_lg_u32 s76, 1\n"
    "s_cbranch_scc1 Lok_71_%=\n"
    "buffer_load_dwordx4 a[32:35], v3, s[36:39], s60 offen offset:0 sc1\n"
    "buffer_load_dwordx4 a[36:39], v3, s[36:39], s61 offen offset:0 sc1\n"
    "Lok_71_%=:\n"
    "s_waitcnt lgkmcnt(12)\n"
    "v_mfma_f32_16x16x32_bf16 v[116:119], a[4:7], v[152:155], v[116:119]\n"
    "ds_read_b64_tr_b16 a[0:1], v2 offset:25344\n"
    "v_mfma_f32_16x16x32_bf16 v[84:87], a[4:7], v[148:151], v[84:87]\n"
    "ds_read_b64_tr_b16 a[2:3], v2 offset:25856\n"
    "v_mfma_f32_16x16x32_bf16 v[52:55], a[4:7], v[144:147], v[52:55]\n"
    "s_waitcnt lgkmcnt(12)\n"
    "v_mfma_f32_16x16x32_bf16 v[56:59], a[8:11], v[144:147], v[56:59]\n"
    "ds_read_b64_tr_b16 a[4:5], v2 offset:25376\n"
    "v_mfma_f32_16x16x32_bf16 v[88:91], a[8:11], v[148:151], v[88:91]\n"
    "ds_read_b64_tr_b16 a[6:7], v2 offset:25888\n"
    "v_mfma_f32_16x16x32_bf16 v[120:123], a[8:11], v[152:155], v[120:123]\n"
    "s_waitcnt lgkmcnt(4)\n"
    "ds_add_u32 v4, v6\n"
    "s_add_u32 s46, s46, 1\n"
    "v_mfma_f32_16x16x32_bf16 v[124:127], a[12:15], v[152:155], v[124:127]\n"
    "ds_read_b64_tr_b16 a[8:9], v2 offset:25408\n"
    "s_bfe_u32 s41, s48, 196616\n"
    "v_mfma_f32_16x16x32_bf16 v[92:95], a[12:15], v[148:151], v[92:95]\n"
    "ds_read_b64_tr_b16 a[10:11], v2 offset:25920\n"
    "s_lshl_b32 s43, s41, 17\n"
    "v_mfma_f32_16x16x32_bf16 v[60:63], a[12:15], v[144:147], v[60:63]\n"
    "s_cmp_lg_u32 s76, 1\n"
    "s_cbranch_scc1 Lok_72_%=\n"
    "buffer_load_dwordx4 a[40:43], v3, s[36:39], s62 offen offset:0 sc1\n"
    "buffer_load_dwordx4 a[44:47], v3, s[36:39], s60 offen offset:1024 sc1\n"
    "Lok_72_%=:\n"
    "v_mfma_f32_16x16x32_bf16 v[64:67], a[16:19], v[144:147], v[64:67]\n"
    "ds_read_b64_tr_b16 a[12:13], v2 offset:25440\n"
    "v_mfma_f32_16x16x32_bf16 v[96:99], a[16:19], v[148:151], v[96:99]\n"
    "ds_read_b64_tr_b16 a[14:15], v2 offset:25952\n"
    "v_mfma_f32_16x16x32_bf16 v[128:131], a[16:19], v[152:155], v[128:131]\n"
    "v_mfma_f32_16x16x32_bf16 v[132:135], a[20:23], v[152:155], v[132:135]\n"
    "ds_read_b64_tr_b16 a[16:17], v2 offset:25472\n"
    "v_mfma_f32_16x16x32_bf16 v[100:103], a[20:23], v[148:151], v[100:103]\n"
    "ds_read_b64_tr_b16 a[18:19], v2 offset:25984\n"
    "ds_read_b32 v7, v5\n"
    "v_mfma_f32_16x16x32_bf16 v[68:71], a[20:23], v[144:147], v[68:71]\n"
    "s_cmp_lg_u32 s76, 1\n"
    "s_cbranch_scc1 Lok_73_%=\n"
    "buffer_load_dwordx4 a[48:51], v3, s[36:39], s61 offen offset:1024 sc1\n"
    "buffer_load_dwordx4 a[52:55], v3, s[36:39], s62 offen offset:1024 sc1\n"
    "Lok_73_%=:\n"
    "v_mfma_f32_16x16x32_bf16 v[72:75], a[24:27], v[144:147], v[72:75]\n"
    "ds_read_b64_tr_b16 a[20:21], v2 offset:25504\n"
    "v_mfma_f32_16x16x32_bf16 v[104:107], a[24:27], v[148:151], v[104:107]\n"
    "ds_read_b64_tr_b16 a[22:23], v2 offset:26016\n"
    "v_mfma_f32_16x16x32_bf16 v[136:139], a[24:27], v[152:155], v[136:139]\n"
    "v_mfma_f32_16x16x32_bf16 v[140:143], a[28:31], v[152:155], v[140:143]\n"
    "ds_read_b64_tr_b16 a[24:25], v2 offset:25536\n"
    "v_mfma_f32_16x16x32_bf16 v[108:111], a[28:31], v[148:151], v[108:111]\n"
    "s_waitcnt lgkmcnt(14)\n"
    "ds_read_b64_tr_b16 a[26:27], v2 offset:26048\n"
    "s_waitcnt lgkmcnt(4)\n"
    "v_cmp_gt_u32 vcc, s46, v7\n"
    "s_add_u32 m0, s40, 16896\n"
    "v_mfma_f32_16x16x32_bf16 v[76:79], a[28:31], v[144:147], v[76:79]\n"
    "s_cbranch_vccnz Lfs_78_%=\n"
    "Lfb_77_%=:\n"
    "s_cmp_ge_u32 s44, s77\n"
    "s_cbranch_scc1 Lwk_79_%=\n"
    "buffer_load_dwordx4 v19, s[28:31], s43 offen lds\n"
    "s_add_u32 m0, s40, 16928\n"
    "s_nop 0\n"
    "buffer_load_dwordx4 v19, s[28:31], s43 offen offset:1024 lds\n"
    "s_add_u32 s43, s43, 32768\n"
    "Lwk_79_%=:\n"
    "v_mfma_f32_16x16x32_bf16 v[48:51], a[0:3], v[156:159], v[48:51]\n"
    "ds_read_b64_tr_b16 a[28:29], v2 offset:25568\n"
    "v_mfma_f32_16x16x32_bf16 v[80:83], a[0:3], v[160:163], v[80:83]\n"
    "ds_read_b64_tr_b16 a[30:31], v2 offset:26080\n"
    "v_mfma_f32_16x16x32_bf16 v[112:115], a[0:3], v[164:167], v[112:115]\n"
    "s_cmp_lg_u32 s76, 1\n"
    "s_cbranch_scc1 Lok_74_%=\n"
    "buffer_load_dwordx4 a[56:59], v3, s[36:39], s60 offen offset:2048 sc1\n"
    "buffer_load_dwordx4 a[60:63], v3, s[36:39], s61 offen offset:2048 sc1\n"
    "Lok_74_%=:\n"
    "v_mfma_f32_16x16x32_bf16 v[116:119], a[4:7], v[164:167], v[116:119]\n"
    "ds_read_b64_tr_b16 a[0:1], v2 offset:42240\n"
    "v_mfma_f32_16x16x32_bf16 v[84:87], a[4:7], v[160:163], v[84:87]\n"
    "ds_read_b64_tr_b16 a[2:3], v2 offset:42752\n"
    "v_mfma_f32_16x16x32_bf16 v[52:55], a[4:7], v[156:159], v[52:55]\n"
    "v_mfma_f32_16x16x32_bf16 v[56:59], a[8:11], v[156:159], v[56:59]\n"
    "ds_read_b64_tr_b16 a[4:5], v2 offset:42272\n"
    "v_mfma_f32_16x16x32_bf16 v[88:91], a[8:11], v[160:163], v[88:91]\n"
    "ds_read_b64_tr_b16 a[6:7], v2 offset:42784\n"
    "v_mfma_f32_16x16x32_bf16 v[120:123], a[8:11], v[164:167], v[120:123]\n"
    "s_waitcnt lgkmcnt(4)\n"
    "ds_add_u32 v4, v6\n"
    "s_add_u32 s46, s46, 1\n"
    "v_mfma_f32_16x16x32_bf16 v[124:127], a[12:15], v[164:167], v[124:127]\n"
    "ds_read_b64_tr_b16 a[8:9], v2 offset:42304\n"
    "v_mfma_f32_16x16x32_bf16 v[92:95], a[12:15], v[160:163], v[92:95]\n"
    "ds_read_b64_tr_b16 a[10:11], v2 offset:42816\n"
    "v_mfma_f32_16x16x32_bf16 v[60:63], a[12:15], v[156:159], v[60:63]\n"
    "s_cmp_lg_u32 s76, 1\n"
    "s_cbranch_scc1 Lok_75_%=\n"
    "buffer_load_dwordx4 v[24:27], v3, s[36:39], s62 offen offset:2048 sc1\n"
    "buffer_load_dwordx4 v[28:31], v3, s[36:39], s60 offen offset:3072 sc1\n"
    "Lok_75_%=:\n"
    "v_mfma_f32_16x16x32_bf16 v[64:67], a[16:19], v[156:159], v[64:67]\n"
    "ds_read_b64_tr_b16 a[12:13], v2 offset:42336\n"
    "v_mfma_f32_16x16x32_bf16 v[96:99], a[16:19], v[160:163], v[96:99]\n"
    "ds_read_b64_tr_b16 a[14:15], v2 offset:42848\n"
    "v_mfma_f32_16x16x32_bf16 v[128:131], a[16:19], v[164:167], v[128:131]\n"
    "v_mfma_f32_16x16x32_bf16 v[132:135], a[20:23], v[164:167], v[132:135]\n"
    "ds_read_b64_tr_b16 a[16:17], v2 offset:42368\n"
    "v_mfma_f32_16x16x32_bf16 v[100:103], a[20:23], v[160:163], v[100:103]\n"
    "ds_read_b64_tr_b16 a[18:19], v2 offset:42880\n"
    "ds_read_b32 v7, v5\n"
    "v_mfma_f32_16x16x32_bf16 v[68:71], a[20:23], v[156:159], v[68:71]\n"
    "s_cmp_lg_u32 s76, 1\n"
    "s_cbranch_scc1 Lok_76_%=\n"
    "buffer_load_dwordx4 v[32:35], v3, s[36:39], s61 offen offset:3072 sc1\n"
    "buffer_load_dwordx4 v[36:39], v3, s[36:39], s62 offen offset:3072 sc1\n"
    "Lok_76_%=:\n"
    "v_mfma_f32_16x16x32_bf16 v[72:75], a[24:27], v[156:159], v[72:75]\n"
    "ds_read_b64_tr_b16 a[20:21], v2 offset:42400\n"
    "v_mfma_f32_16x16x32_bf16 v[104:107], a[24:27], v[160:163], v[104:107]\n"
    "ds_read_b64_tr_b16 a[22:23], v2 offset:42912\n"
    "v_mfma_f32_16x16x32_bf16 v[136:139], a[24:27], v[164:167], v[136:139]\n"
    "v_mfma_f32_16x16x32_bf16 v[140:143], a[28:31], v[164:167], v[140:143]\n"
    "ds_read_b64_tr_b16 a[24:25], v2 offset:42432\n"
    "v_mfma_f32_16x16x32_bf16 v[108:111], a[28:31], v[160:163], v[108:111]\n"
    "s_waitcnt lgkmcnt(14)\n"
    "ds_read_b64_tr_b16 a[26:27], v2 offset:42944\n"
    "s_waitcnt lgkmcnt(4)\n"
    "v_cmp_gt_u32 vcc, s46, v7\n"
    "s_add_u32 m0, s40, 25344\n"
    "v_mfma_f32_16x16x32_bf16 v[76:79], a[28:31], v[156:159], v[76:79]\n"
    "s_cbranch_vccnz Lfs_81_%=\n"
    "Lfb_80_%=:\n"
    "s_cmp_ge_u32 s44, s77\n"
    "s_cbranch_scc1 Lwk_82_%=\n"
    "buffer_load_dwordx4 v19, s[28:31], s43 offen lds\n"
    "s_add_u32 m0, s40, 25376\n"
    "s_nop 0\n"
    "buffer_load_dwordx4 v19, s[28:31], s43 offen offset:1024 lds\n"
    "s_add_u32 s43, s43, 32768\n"
    "Lwk_82_%=:\n"
    "v_mfma_f32_16x16x32_bf16 v[48:51], a[0:3], v[168:171], v[48:51]\n"
    "ds_read_b64_tr_b16 a[28:29], v2 offset:42464\n"
    "s_and_b32 s54, s84, 15\n"
    "s_cbranch_scc0 Lpk_89_%=\n"
    "global_load_dword v16, v12, %[ws] sc1\n"
    "Lpk_89_%=:\n"
    "v_mfma_f32_16x16x32_bf16 v[80:83], a[0:3], v[172:175], v[80:83]\n"
    "ds_read_b64_tr_b16 a[30:31], v2 offset:42976\n"
    "v_mfma_f32_16x16x32_bf16 v[112:115], a[0:3], v[176:179], v[112:115]\n"
    "v_mfma_f32_16x16x32_bf16 v[116:119], a[4:7], v[176:179], v[116:119]\n"
    "ds_read_b64_tr_b16 a[0:1], v2 offset:33792\n"
    "v_mfma_f32_16x16x32_bf16 v[84:87], a[4:7], v[172:175], v[84:87]\n"
    "ds_read_b64_tr_b16 a[2:3], v2 offset:34304\n"
    "v_mfma_f32_16x16x32_bf16 v[52:55], a[4:7], v[168:171], v[52:55]\n"
    "v_mfma_f32_16x16x32_bf16 v[56:59], a[8:11], v[168:171], v[56:59]\n"
    "ds_read_b64_tr_b16 a[4:5], v2 offset:33824\n"
    "v_mfma_f32_16x16x32_bf16 v[88:91], a[8:11], v[172:175], v[88:91]\n"
    "ds_read_b64_tr_b16 a[6:7], v2 offset:34336\n"
    "v_mfma_f32_16x16x32_bf16 v[120:123], a[8:11], v[176:179], v[120:123]\n"
    "s_waitcnt lgkmcnt(4)\n"
    "ds_add_u32 v4, v6\n"
    "s_add_u32 s46, s46, 1\n"
    "v_mfma_f32_16x16x32_bf16 v[124:127], a[12:15], v[176:179], v[124:127]\n"
    "ds_read_b64_tr_b16 a[8:9], v2 offset:33856\n"
    "v_mfma_f32_16x16x32_bf16 v[92:95], a[12:15], v[172:175], v[92:95]\n"
    "ds_read_b64_tr_b16 a[10:11], v2 offset:34368\n"
    "v_mfma_f32_16x16x32_bf16 v[60:63], a[12:15], v[168:171], v[60:63]\n"
    "v_mfma_f32_16x16x32_bf16 v[64:67], a[16:19], v[168:171], v[64:67]\n"
    "ds_read_b64_tr_b16 a[12:13], v2 offset:33888\n"
    "v_mfma_f32_16x16x32_bf16 v[96:99], a[16:19], v[172:175], v[96:99]\n"
    "ds_read_b64_tr_b16 a[14:15], v2 offset:34400\n"
    "v_mfma_f32_16x16x32_bf16 v[128:131], a[16:19], v[176:179], v[128:131]\n"
    "s_bfe_u32 s41, s48, 196616\n"
    "s_mul_i32 s60, s41, 49152\n"
    "v_mfma_f32_16x16x32_bf16 v[132:135], a[20:23], v[176:179], v[132:135]\n"
    "ds_read_b64_tr_b16 a[16:17], v2 offset:33920\n"
    "v_mfma_f32_16x16x32_bf16 v[100:103], a[20:23], v[172:175], v[100:103]\n"
    "ds_read_b64_tr_b16 a[18:19], v2 offset:34432\n"
    "ds_read_b32 v7, v5\n"
    "v_mfma_f32_16x16x32_bf16 v[68:71], a[20:23], v[168:171], v[68:71]\n"
    "s_add_u32 s61, s60, 4096\n"
    "s_add_u32 s62, s60, 8192\n"
    "v_mfma_f32_16x16x32_bf16 v[72:75], a[24:27], v[168:171], v[72:75]\n"
    "ds_read_b64_tr_b16 a[20:21], v2 offset:33952\n"
    "v_mfma_f32_16x16x32_bf16 v[104:107], a[24:27], v[172:175], v[104:107]\n"
    "ds_read_b64_tr_b16 a[22:23], v2 offset:34464\n"
    "v_mfma_f32_16x16x32_bf16 v[136:139], a[24:27], v[176:179], v[136:139]\n"
    "v_mfma_f32_16x16x32_bf16 v[140:143], a[28:31], v[176:179], v[140:143]\n"
    "ds_read_b64_tr_b16 a[24:25], v2 offset:33984\n"
    "v_mfma_f32_16x16x32_bf16 v[108:111], a[28:31], v[172:175], v[108:111]\n"
    "s_waitcnt lgkmcnt(14)\n"
    "ds_read_b64_tr_b16 a[26:27], v2 offset:34496\n"
    "s_waitcnt lgkmcnt(4)\n"
    "v_cmp_gt_u32 vcc, s46, v7\n"
    "s_add_u32 m0, s40, 42240\n"
    "v_mfma_f32_16x16x32_bf16 v[76:79], a[28:31], v[168:171], v[76:79]\n"
    "s_cbranch_vccnz Lfs_84_%=\n"
    "Lfb_83_%=:\n"
    "s_cmp_ge_u32 s44, s77\n"
    "s_cbranch_scc1 Lwk_85_%=\n"
    "buffer_load_dwordx4 v19, s[28:31], s43 offen lds\n"
    "s_add_u32 m0, s40, 42272\n"
    "s_nop 0\n"
    "buffer_load_dwordx4 v19, s[28:31], s43 offen offset:1024 lds\n"
    "s_add_u32 s43, s43, 32768\n"
    "Lwk_85_%=:\n"
    "v_mfma_f32_16x16x32_bf16 v[48:51], a[0:3], v[180:183], v[48:51]\n"
    "ds_read_b64_tr_b16 a[28:29], v2 offset:34016\n"
    "v_mfma_f32_16x16x32_bf16 v[80:83], a[0:3], v[184:187], v[80:83]\n"
    "ds_read_b64_tr_b16 a[30:31], v2 offset:34528\n"
    "v_mfma_f32_16x16x32_bf16 v[112:115], a[0:3], v[188:191], v[112:115]\n"
    "v_mfma_f32_16x16x32_bf16 v[116:119], a[4:7], v[188:191], v[116:119]\n"
    "ds_read_b64_tr_b16 a[0:1], v2 offset:50688\n"
    "v_mfma_f32_16x16x32_bf16 v[84:87], a[4:7], v[184:187], v[84:87]\n"
    "ds_read_b64_tr_b16 a[2:3], v2 offset:51200\n"
    "v_mfma_f32_16x16x32_bf16 v[52:55], a[4:7], v[180:183], v[52:55]\n"
    "v_mfma_f32_16x16x32_bf16 v[56:59], a[8:11], v[180:183], v[56:59]\n"
    "ds_read_b64_tr_b16 a[4:5], v2 offset:50720\n"
    "v_mfma_f32_16x16x32_bf16 v[88:91], a[8:11], v[184:187], v[88:91]\n"
    "ds_read_b64_tr_b16 a[6:7], v2 offset:51232\n"
    "v_mfma_f32_16x16x32_bf16 v[120:123], a[8:11], v[188:191], v[120:123]\n"
    "s_waitcnt lgkmcnt(4)\n"
    "ds_add_u32 v4, v6\n"
    "s_add_u32 s46, s46, 1\n"
    "v_mfma_f32_16x16x32_bf16 v[124:127], a[12:15], v[188:191], v[124:127]\n"
    "ds_read_b64_tr_b16 a[8:9], v2 offset:50752\n"
    "v_mfma_f32_16x16x32_bf16 v[92:95], a[12:15], v[184:187], v[92:95]\n"
    "ds_read_b64_tr_b16 a[10:11], v2 offset:51264\n"
    "v_mfma_f32_16x16x32_bf16 v[60:63], a[12:15], v[180:183], v[60:63]\n"
    "v_mfma_f32_16x16x32_bf16 v[64:67], a[16:19], v[180:183], v[64:67]\n"
    "ds_read_b64_tr_b16 a[12:13], v2 offset:50784\n"
    "v_mfma_f32_16x16x32_bf16 v[96:99], a[16:19], v[184:187], v[96:99]\n"
    "ds_read_b64_tr_b16 a[14:15], v2 offset:51296\n"
    "v_mfma_f32_16x16x32_bf16 v[128:131], a[16:19], v[188:191], v[128:131]\n"
    "v_mfma_f32_16x16x32_bf16 v[132:135], a[20:23], v[188:191], v[132:135]\n"
    "ds_read_b64_tr_b16 a[16:17], v2 offset:50816\n"
    "v_mfma_f32_16x16x32_bf16 v[100:103], a[20:23], v[184:187], v[100:103]\n"
    "ds_read_b64_tr_b16 a[18:19], v2 offset:51328\n"
    "ds_read_b32 v7, v5\n"
    "v_mfma_f32_16x16x32_bf16 v[68:71], a[20:23], v[180:183], v[68:71]\n"
    "s_waitcnt vmcnt(0)\n"
    "v_bfi_b32 v17, v15, v14, v16\n"
    "v_cmp_eq_u32 vcc, v17, v16\n"
    "v_mfma_f32_16x16x32_bf16 v[72:75], a[24:27], v[180:183], v[72:75]\n"
    "ds_read_b64_tr_b16 a[20:21], v2 offset:50848\n"
    "v_cmp_ne_u32 s[54:55], v17, v18\n"
    "v_mfma_f32_16x16x32_bf16 v[104:107], a[24:27], v[184:187], v[104:107]\n"
    "ds_read_b64_tr_b16 a[22:23], v2 offset:51360\n"
    "s_xor_b32 s55, vcc_lo, 0xff00\n"
    "s_or_b32 s54, s54, 0xff\n"
    "v_mfma_f32_16x16x32_bf16 v[136:139], a[24:27], v[188:191], v[136:139]\n"
    "s_and_b32 s54, s54, s55\n"
    "s_and_b32 s54, s54, s84\n"
    "s_cbranch_scc1 Lhs_91_%=\n"
    "Lhb_90_%=:\n"
    "s_andn2_b32 s84, s84, vcc_lo\n"
    "v_mfma_f32_16x16x32_bf16 v[140:143], a[28:31], v[188:191], v[140:143]\n"
    "ds_read_b64_tr_b16 a[24:25], v2 offset:50880\n"
    "v_mfma_f32_16x16x32_bf16 v[108:111], a[28:31], v[184:187], v[108:111]\n"
    "s_waitcnt lgkmcnt(14)\n"
    "ds_read_b64_tr_b16 a[26:27], v2 offset:51392\n"
    "s_waitcnt lgkmcnt(4)\n"
    "v_cmp_gt_u32 vcc, s46, v7\n"
    "s_add_u32 m0, s40, 33792\n"
    "s_cmp_eq_u32 %[team], 0\n"
    "s_cbranch_scc1 Lpu_92_%=\n"
    "s_setprio 1\n"
    "Lpu_92_%=:\n"
    "v_mfma_f32_16x16x32_bf16 v[76:79], a[28:31], v[180:183], v[76:79]\n"
    "s_cbranch_vccnz Lfs_87_%=\n"
    "Lfb_86_%=:\n"
    "s_cmp_ge_u32 s44, s77\n"
    "s_cbranch_scc1 Lwk_88_%=\n"
    "buffer_load_dwordx4 v19, s[28:31], s43 offen lds\n"
    "s_add_u32 m0, s40, 33824\n"
    "s_nop 0\n"
    "buffer_load_dwordx4 v19, s[28:31], s43 offen offset:1024 lds\n"
    "s_add_u32 s43, s43, 32768\n"
    "Lwk_88_%=:\n"
    "s_add_u32 s44, s44, 1\n"
    "s_lshr_b32 s48, s48, 4\n"
    "s_cmp_ge_u32 s44, s42\n"
    "s_cbranch_scc1 Lo3_%=\n"
    "Ltop1_%=:\n"
    "s_cmp_eq_u32 s45, 0\n"
    "s_cbranch_scc1 Lst1_%=\n"
    "Lgo1_%=:\n"
    "s_or_b32 s54, s84, s78\n"
    "s_bitcmp0_b32 s54, s44\n"
    "s_cselect_b32 s76, 1, 0\n"
    "s_cselect_b32 s45, 1, 0\n"
    "Lgp1_%=:\n"
    "v_mfma_f32_16x16x32_bf16 v[48:51], a[0:3], a[32:35], v[48:51]\n"
    "ds_read_b64_tr_b16 a[28:29], v2 offset:50912\n"
    "v_mfma_f32_16x16x32_bf16 v[80:83], a[0:3], a[36:39], v[80:83]\n"
    "ds_read_b64_tr_b16 a[30:31], v2 offset:51424\n"
    "v_mfma_f32_16x16x32_bf16 v[112:115], a[0:3], a[40:43], v[112:115]\n"
    "s_cmp_lg_u32 s76, 1\n"
    "s_cbranch_scc1 Lok_93_%=\n"
    "buffer_load_dwordx4 v[144:147], v3, s[36:39], s60 offen offset:0 sc1\n"
    "buffer_load_dwordx4 v[148:151], v3, s[36:39], s61 offen offset:0 sc1\n"
    "Lok_93_%=:\n"
    "v_mfma_f32_16x16x32_bf16 v[116:119], a[4:7], a[40:43], v[116:119]\n"
    "ds_read_b64_tr_b16 a[0:1], v2 offset:0\n"
    "v_mfma_f32_16x16x32_bf16 v[84:87], a[4:7], a[36:39], v[84:87]\n"
    "ds_read_b64_tr_b16 a[2:3], v2 offset:512\n"
    "v_mfma_f32_16x16x32_bf16 v[52:55], a[4:7], a[32:35], v[52:55]\n"
    "v_mfma_f32_16x16x32_bf16 v[56:59], a[8:11], a[32:35], v[56:59]\n"
    "ds_read_b64_tr_b16 a[4:5], v2 offset:32\n"
    "v_mfma_f32_16x16x32_bf16 v[88:91], a[8:11], a[36:39], v[88:91]\n"
    "ds_read_b64_tr_b16 a[6:7], v2 offset:544\n"
    "v_mfma_f32_16x16x32_bf16 v[120:123], a[8:11], a[40:43], v[120:123]\n"
    "s_waitcnt lgkmcnt(4)\n"
    "ds_add_u32 v4, v6\n"
    "s_add_u32 s46, s46, 1\n"
    "v_mfma_f32_16x16x32_bf16 v[124:127], a[12:15], a[40:43], v[124:127]\n"
    "ds_read_b64_tr_b16 a[8:9], v2 offset:64\n"
    "s_bfe_u32 s41, s48, 196616\n"
    "v_mfma_f32_16x16x32_bf16 v[92:95], a[12:15], a[36:39], v[92:95]\n"
    "ds_read_b64_tr_b16 a[10:11], v2 offset:576\n"
    "s_lshl_b32 s43, s41, 17\n"
    "v_mfma_f32_16x16x32_bf16 v[60:63], a[12:15], a[32:35], v[60:63]\n"
    "s_cmp_lg_u32 s76, 1\n"
    "s_cbranch_scc1 Lok_94_%=\n"
    "buffer_load_dwordx4 v[152:155], v3, s[36:39], s62 offen offset:0 sc1\n"
    "buffer_load_dwordx4 v[156:159], v3, s[36:39], s60 offen offset:1024 sc1\n"
    "Lok_94_%=:\n"
    "v_mfma_f32_16x16x32_bf16 v[64:67], a[16:19], a[32:35], v[64:67]\n"
    "ds_read_b64_tr_b16 a[12:13], v2 offset:96\n"
    "v_mfma_f32_16x16x32_bf16 v[96:99], a[16:19], a[36:39], v[96:99]\n"
    "ds_read_b64_tr_b16 a[14:15], v2 offset:608\n"
    "v_mfma_f32_16x16x32_bf16 v[128:131], a[16:19], a[40:43], v[128:131]\n"
    "v_mfma_f32_16x16x32_bf16 v[132:135], a[20:23], a[40:43], v[132:135]\n"
    "ds_read_b64_tr_b16 a[16:17], v2 offset:128\n"
    "v_mfma_f32_16x16x32_bf16 v[100:103], a[20:23], a[36:39], v[100:103]\n"
    "ds_read_b64_tr_b16 a[18:19], v2 offset:640\n"
    "ds_read_b32 v7, v5\n"
    "v_mfma_f32_16x16x32_bf16 v[68:71], a[20:23], a[32:35], v[68:71]\n"
    "s_cmp_lg_u32 s76, 1\n"
    "s_cbranch_scc1 Lok_95_%=\n"
    "buffer_load_dwordx4 v[160:163], v3, s[36:39], s61 offen offset:1024 sc1\n"
    "buffer_load_dwordx4 v[164:167], v3, s[36:39], s62 offen offset:1024 sc1\n"
    "Lok_95_%=:\n"
    "v_mfma_f32_16x16x32_bf16 v[72:75], a[24:27], a[32:35], v[72:75]\n"
    "ds_read_b64_tr_b16 a[20:21], v2 offset:160\n"
    "v_mfma_f32_16x16x32_bf16 v[104:107], a[24:27], a[36:39], v[104:107]\n"
    "ds_read_b64_tr_b16 a[22:23], v2 offset:672\n"
    "v_mfma_f32_16x16x32_bf16 v[136:139], a[24:27], a[40:43], v[136:139]\n"
    "v_mfma_f32_16x16x32_bf16 v[140:143], a[28:31], a[40:43], v[140:143]\n"
    "ds_read_b64_tr_b16 a[24:25], v2 offset:192\n"
    "v_mfma_f32_16x16x32_bf16 v[108:111], a[28:31], a[36:39], v[108:111]\n"
    "s_waitcnt lgkmcnt(14)\n"
    "ds_read_b64_tr_b16 a[26:27], v2 offset:704\n"
    "s_waitcnt lgkmcnt(4)\n"
    "v_cmp_gt_u32 vcc, s46, v7\n"
    "s_add_u32 m0, s40, 50688\n"
    "v_mfma_f32_16x16x32_bf16 v[76:79], a[28:31], a[32:35], v[76:79]\n"
    "s_cbranch_vccnz Lfs_100_%=\n"
    "Lfb_99_%=:\n"
    "s_cmp_ge_u32 s44, s77\n"
    "s_cbranch_scc1 Lwk_101_%=\n"
    "buffer_load_dwordx4 v19, s[28:31], s43 offen lds\n"
    "s_add_u32 m0, s40, 50720\n"
    "s_nop 0\n"
    "buffer_load_dwordx4 v19, s[28:31], s43 offen offset:1024 lds\n"
    "s_add_u32 s43, s43, 32768\n"
    "Lwk_101_%=:\n"
    "v_mfma_f32_16x16x32_bf16 v[48:51], a[0:3], a[44:47], v[48:51]\n"
    "ds_read_b64_tr_b16 a[28:29], v2 offset:224\n"
    "v_mfma_f32_16x16x32_bf16 v[80:83], a[0:3], a[48:51], v[80:83]\n"
    "ds_read_b64_tr_b16 a[30:31], v2 offset:736\n"
    "v_mfma_f32_16x16x32_bf16 v[112:115], a[0:3], a[52:55], v[112:115]\n"
    "s_cmp_lg_u32 s76, 1\n"
    "s_cbranch_scc1 Lok_96_%=\n"
    "buffer_load_dwordx4 v[168:171], v3, s[36:39], s60 offen offset:2048 sc1\n"
    "buffer_load_dwordx4 v[172:175], v3, s[36:39], s61 offen offset:2048 sc1\n"
    "Lok_96_%=:\n"
    "v_mfma_f32_16x16x32_bf16 v[116:119], a[4:7], a[52:55], v[116:119]\n"
    "ds_read_b64_tr_b16 a[0:1], v2 offset:59136\n"
    "v_mfma_f32_16x16x32_bf16 v[84:87], a[4:7], a[48:51], v[84:87]\n"
    "ds_read_b64_tr_b16 a[2:3], v2 offset:59648\n"
    "v_mfma_f32_16x16x32_bf16 v[52:55], a[4:7], a[44:47], v[52:55]\n"
    "v_mfma_f32_16x16x32_bf16 v[56:59], a[8:11], a[44:47], v[56:59]\n"
    "ds_read_b64_tr_b16 a[4:5], v2 offset:59168\n"
    "v_mfma_f32_16x16x32_bf16 v[88:91], a[8:11], a[48:51], v[88:91]\n"
    "ds_read_b64_tr_b16 a[6:7], v2 offset:59680\n"
    "v_mfma_f32_16x16x32_bf16 v[120:123], a[8:11], a[52:55], v[120:123]\n"
    "s_waitcnt lgkmcnt(4)\n"
    "ds_add_u32 v4, v6\n"
    "s_add_u32 s46, s46, 1\n"
    "v_mfma_f32_16x16x32_bf16 v[124:127], a[12:15], a[52:55], v[124:127]\n"
    "ds_read_b64_tr_b16 a[8:9], v2 offset:59200\n"
    "v_mfma_f32_16x16x32_bf16 v[92:95], a[12:15], a[48:51], v[92:95]\n"
    "ds_read_b64_tr_b16 a[10:11], v2 offset:59712\n"
    "v_mfma_f32_16x16x32_bf16 v[60:63], a[12:15], a[44:47], v[60:63]\n"
    "s_cmp_lg_u32 s76, 1\n"
    "s_cbranch_scc1 Lok_97_%=\n"
    "buffer_load_dwordx4 v[176:179], v3, s[36:39], s62 offen offset:2048 sc1\n"
    "buffer_load_dwordx4 v[180:183], v3, s[36:39], s60 offen offset:3072 sc1\n"
    "Lok_97_%=:\n"
    "v_mfma_f32_16x16x32_bf16 v[64:67], a[16:19], a[44:47], v[64:67]\n"
    "ds_read_b64_tr_b16 a[12:13], v2 offset:59232\n"
    "v_mfma_f32_16x16x32_bf16 v[96:99], a[16:19], a[48:51], v[96:99]\n"
    "ds_read_b64_tr_b16 a[14:15], v2 offset:59744\n"
    "v_mfma_f32_16x16x32_bf16 v[128:131], a[16:19], a[52:55], v[128:131]\n"
    "v_mfma_f32_16x16x32_bf16 v[132:135], a[20:23], a[52:55], v[132:135]\n"
    "ds_read_b64_tr_b16 a[16:17], v2 offset:59264\n"
    "v_mfma_f32_16x16x32_bf16 v[100:103], a[20:23], a[48:51], v[100:103]\n"
    "ds_read_b64_tr_b16 a[18:19], v2 offset:59776\n"
    "ds_read_b32 v7, v5\n"
    "v_mfma_f32_16x16x32_bf16 v[68:71], a[20:23], a[44:47], v[68:71]\n"
    "s_cmp_lg_u32 s76, 1\n"
    "s_cbranch_scc1 Lok_98_%=\n"
    "buffer_load_dwordx4 v[184:187], v3, s[36:39], s61 offen offset:3072 sc1\n"
    "buffer_load_dwordx4 v[188:191], v3, s[36:39], s62 offen offset:3072 sc1\n"
    "Lok_98_%=:\n"
    "v_mfma_f32_16x16x32_bf16 v[72:75], a[24:27], a[44:47], v[72:75]\n"
    "ds_read_b64_tr_b16 a[20:21], v2 offset:59296\n"
    "v_mfma_f32_16x16x32_bf16 v[104:107], a[24:27], a[48:51], v[104:107]\n"
    "ds_read_b64_tr_b16 a[22:23], v2 offset:59808\n"
    "v_mfma_f32_16x16x32_bf16 v[136:139], a[24:27], a[52:55], v[136:139]\n"
    "v_mfma_f32_16x16x32_bf16 v[140:143], a[28:31], a[52:55], v[140:143]\n"
    "ds_read_b64_tr_b16 a[24:25], v2 offset:59328\n"
    "v_mfma_f32_16x16x32_bf16 v[108:111], a[28:31], a[48:51], v[108:111]\n"
    "s_waitcnt lgkmcnt(14)\n"
    "ds_read_b64_tr_b16 a[26:27], v2 offset:59840\n"
    "s_waitcnt lgkmcnt(4)\n"
    "v_cmp_gt_u32 vcc, s46, v7\n"
    "s_add_u32 m0, s40, 0\n"
    "v_mfma_f32_16x16x32_bf16 v[76:79], a[28:31], a[44:47], v[76:79]\n"
    "s_cbranch_vccnz Lfs_103_%=\n"
    "Lfb_102_%=:\n"
    "s_cmp_ge_u32 s44, s77\n"
    "s_cbranch_scc1 Lwk_104_%=\n"
    "buffer_load_dwordx4 v19, s[28:31], s43 offen lds\n"
    "s_add_u32 m0, s40, 32\n"
    "s_nop 0\n"
    "buffer_load_dwordx4 v19, s[28:31], s43 offen offset:1024 lds\n"
    "s_add_u32 s43, s43, 32768\n"
    "Lwk_104_%=:\n"
    "v_mfma_f32_16x16x32_bf16 v[48:51], a[0:3], a[56:59], v[48:51]\n"
    "ds_read_b64_tr_b16 a[28:29], v2 offset:59360\n"
    "s_and_b32 s54, s84, 15\n"
    "s_cbranch_scc0 Lpk_111_%=\n"
    "global_load_dword v16, v12, %[ws] sc1\n"
    "Lpk_111_%=:\n"
    "v_mfma_f32_16x16x32_bf16 v[80:83], a[0:3], a[60:63], v[80:83]\n"
    "ds_read_b64_tr_b16 a[30:31], v2 offset:59872\n"
    "v_mfma_f32_16x16x32_bf16 v[112:115], a[0:3], v[24:27], v[112:115]\n"
    "v_mfma_f32_16x16x32_bf16 v[116:119], a[4:7], v[24:27], v[116:119]\n"
    "ds_read_b64_tr_b16 a[0:1], v2 offset:8448\n"
    "v_mfma_f32_16x16x32_bf16 v[84:87], a[4:7], a[60:63], v[84:87]\n"
    "ds_read_b64_tr_b16 a[2:3], v2 offset:8960\n"
    "v_mfma_f32_16x16x32_bf16 v[52:55], a[4:7], a[56:59], v[52:55]\n"
    "v_mfma_f32_16x16x32_bf16 v[56:59], a[8:11], a[56:59], v[56:59]\n"
    "ds_read_b64_tr_b16 a[4:5], v2 offset:8480\n"
    "v_mfma_f32_16x16x32_bf16 v[88:91], a[8:11], a[60:63], v[88:91]\n"
    "ds_read_b64_tr_b16 a[6:7], v2 offset:8992\n"
    "v_mfma_f32_16x16x32_bf16 v[120:123], a[8:11], v[24:27], v[120:123]\n"
    "s_waitcnt lgkmcnt(4)\n"
    "ds_add_u32 v4, v6\n"
    "s_add_u32 s46, s46, 1\n"
    "v_mfma_f32_16x16x32_bf16 v[124:127], a[12:15], v[24:27], v[124:127]\n"
    "ds_read_b64_tr_b16 a[8:9], v2 offset:8512\n"
    "v_mfma_f32_16x16x32_bf16 v[92:95], a[12:15], a[60:63], v[92:95]\n"
    "ds_read_b64_tr_b16 a[10:11], v2 offset:9024\n"
    "v_mfma_f32_16x16x32_bf16 v[60:63], a[12:15], a[56:59], v[60:63]\n"
    "v_mfma_f32_16x16x32_bf16 v[64:67], a[16:19], a[56:59], v[64:67]\n"
    "ds_read_b64_tr_b16 a[12:13], v2 offset:8544\n"
    "v_mfma_f32_16x16x32_bf16 v[96:99], a[16:19], a[60:63], v[96:99]\n"
    "ds_read_b64_tr_b16 a[14:15], v2 offset:9056\n"
    "v_mfma_f32_16x16x32_bf16 v[128:131], a[16:19], v[24:27], v[128:131]\n"
    "s_bfe_u32 s41, s48, 196616\n"
    "s_mul_i32 s60, s41, 49152\n"
    "v_mfma_f32_16x16x32_bf16 v[132:135], a[20:23], v[24:27], v[132:135]\n"
    "ds_read_b64_tr_b16 a[16:17], v2 offset:8576\n"
    "v_mfma_f32_16x16x32_bf16 v[100:103], a[20:23], a[60:63], v[100:103]\n"
    "ds_read_b64_tr_b16 a[18:19], v2 offset:9088\n"
    "ds_read_b32 v7, v5\n"
    "v_mfma_f32_16x16x32_bf16 v[68:71], a[20:23], a[56:59], v[68:71]\n"
    "s_add_u32 s61, s60, 4096\n"
    "s_add_u32 s62, s60, 8192\n"
    "v_mfma_f32_16x16x32_bf16 v[72:75], a[24:27], a[56:59], v[72:75]\n"
    "ds_read_b64_tr_b16 a[20:21], v2 offset:8608\n"
    "v_mfma_f32_16x16x32_bf16 v[104:107], a[24:27], a[60:63], v[104:107]\n"
    "ds_read_b64_tr_b16 a[22:23], v2 offset:9120\n"
    "v_mfma_f32_16x16x32_bf16 v[136:139], a[24:27], v[24:27], v[136:139]\n"
    "v_mfma_f32_16x16x32_bf16 v[140:143], a[28:31], v[24:27], v[140:143]\n"
    "ds_read_b64_tr_b16 a[24:25], v2 offset:8640\n"
    "v_mfma_f32_16x16x32_bf16 v[108:111], a[28:31], a[60:63], v[108:111]\n"
    "s_waitcnt lgkmcnt(14)\n"
    "ds_read_b64_tr_b16 a[26:27], v2 offset:9152\n"
    "s_waitcnt lgkmcnt(4)\n"
    "v_cmp_gt_u32 vcc, s46, v7\n"
    "s_add_u32 m0, s40, 59136\n"
    "v_mfma_f32_16x16x32_bf16 v[76:79], a[28:31], a[56:59], v[76:79]\n"
    "s_cbranch_vccnz Lfs_106_%=\n"
    "Lfb_105_%=:\n"
    "s_cmp_ge_u32 s44, s77\n"
    "s_cbranch_scc1 Lwk_107_%=\n"
    "buffer_load_dwordx4 v19, s[28:31], s43 offen lds\n"
    "s_add_u32 m0, s40, 59168\n"
    "s_nop 0\n"
    "buffer_load_dwordx4 v19, s[28:31], s43 offen offset:1024 lds\n"
    "s_add_u32 s43, s43, 32768\n"
    "Lwk_107_%=:\n"
    "v_mfma_f32_16x16x32_bf16 v[48:51], a[0:3], v[28:31], v[48:51]\n"
    "ds_read_b64_tr_b16 a[28:29], v2 offset:8672\n"
    "v_mfma_f32_16x16x32_bf16 v[80:83], a[0:3], v[32:35], v[80:83]\n"
    "ds_read_b64_tr_b16 a[30:31], v2 offset:9184\n"
    "v_mfma_f32_16x16x32_bf16 v[112:115], a[0:3], v[36:39], v[112:115]\n"
    "v_mfma_f32_16x16x32_bf16 v[116:119], a[4:7], v[36:39], v[116:119]\n"
    "ds_read_b64_tr_b16 a[0:1], v2 offset:16896\n"
    "v_mfma_f32_16x16x32_bf16 v[84:87], a[4:7], v[32:35], v[84:87]\n"
    "ds_read_b64_tr_b16 a[2:3], v2 offset:17408\n"
    "v_mfma_f32_16x16x32_bf16 v[52:55], a[4:7], v[28:31], v[52:55]\n"
    "v_mfma_f32_16x16x32_bf16 v[56:59], a[8:11], v[28:31], v[56:59]\n"
    "ds_read_b64_tr_b16 a[4:5], v2 offset:16928\n"
    "v_mfma_f32_16x16x32_bf16 v[88:91], a[8:11], v[32:35], v[88:91]\n"
    "ds_read_b64_tr_b16 a[6:7], v2 offset:17440\n"
    "v_mfma_f32_16x16x32_bf16 v[120:123], a[8:11], v[36:39], v[120:123]\n"
    "s_waitcnt lgkmcnt(4)\n"
    "ds_add_u32 v4, v6\n"
    "s_add_u32 s46, s46, 1\n"
    "v_mfma_f32_16x16x32_bf16 v[124:127], a[12:15], v[36:39], v[124:127]\n"
    "ds_read_b64_tr_b16 a[8:9], v2 offset:16960\n"
    "v_mfma_f32_16x16x32_bf16 v[92:95], a[12:15], v[32:35], v[92:95]\n"
    "ds_read_b64_tr_b16 a[10:11], v2 offset:17472\n"
    "v_mfma_f32_16x16x32_bf16 v[60:63], a[12:15], v[28:31], v[60:63]\n"
    "v_mfma_f32_16x16x32_bf16 v[64:67], a[16:19], v[28:31], v[64:67]\n"
    "ds_read_b64_tr_b16 a[12:13], v2 offset:16992\n"
    "v_mfma_f32_16x16x32_bf16 v[96:99], a[16:19], v[32:35], v[96:99]\n"
    "ds_read_b64_tr_b16 a[14:15], v2 offset:17504\n"
    "v_mfma_f32_16x16x32_bf16 v[128:131], a[16:19], v[36:39], v[128:131]\n"
    "v_mfma_f32_16x16x32_bf16 v[132:135], a[20:23], v[36:39], v[132:135]\n"
    "ds_read_b64_tr_b16 a[16:17], v2 offset:17024\n"
    "v_mfma_f32_16x16x32_bf16 v[100:103], a[20:23], v[32:35], v[100:103]\n"
    "ds_read_b64_tr_b16 a[18:19], v2 offset:17536\n"
    "ds_read_b32 v7, v5\n"
    "v_mfma_f32_16x16x32_bf16 v[68:71], a[20:23], v[28:31], v[68:71]\n"
    "s_waitcnt vmcnt(0)\n"
    "v_bfi_b32 v17, v15, v14, v16\n"
    "v_cmp_eq_u32 vcc, v17, v16\n"
    "v_mfma_f32_16x16x32_bf16 v[72:75], a[24:27], v[28:31], v[72:75]\n"
    "ds_read_b64_tr_b16 a[20:21], v2 offset:17056\n"
    "v_cmp_ne_u32 s[54:55], v17, v18\n"
    "v_mfma_f32_16x16x32_bf16 v[104:107], a[24:27], v[32:35], v[104:107]\n"
    "ds_read_b64_tr_b16 a[22:23], v2 offset:17568\n"
    "s_xor_b32 s55, vcc_lo, 0xff00\n"
    "s_or_b32 s54, s54, 0xff\n"
    "v_mfma_f32_16x16x32_bf16 v[136:139], a[24:27], v[36:39], v[136:139]\n"
    "s_and_b32 s54, s54, s55\n"
    "s_and_b32 s54, s54, s84\n"
    "s_cbranch_scc1 Lhs_113_%=\n"
    "Lhb_112_%=:\n"
    "s_andn2_b32 s84, s84, vcc_lo\n"
    "v_mfma_f32_16x16x32_bf16 v[140:143], a[28:31], v[36:39], v[140:143]\n"
    "ds_read_b64_tr_b16 a[24:25], v2 offset:17088\n"
    "v_mfma_f32_16x16x32_bf16 v[108:111], a[28:31], v[32:35], v[108:111]\n"
    "s_waitcnt lgkmcnt(14)\n"
    "ds_read_b64_tr_b16 a[26:27], v2 offset:17600\n"
    "s_waitcnt lgkmcnt(4)\n"
    "v_cmp_gt_u32 vcc, s46, v7\n"
    "s_add_u32 m0, s40, 8448\n"
    "v_mfma_f32_16x16x32_bf16 v[76:79], a[28:31], v[28:31], v[76:79]\n"
    "s_cbranch_vccnz Lfs_109_%=\n"
    "Lfb_108_%=:\n"
    "s_cmp_ge_u32 s44, s77\n"
    "s_cbranch_scc1 Lwk_110_%=\n"
    "buffer_load_dwordx4 v19, s[28:31], s43 offen lds\n"
    "s_add_u32 m0, s40, 8480\n"
    "s_nop 0\n"
    "buffer_load_dwordx4 v19, s[28:31], s43 offen offset:1024 lds\n"
    "s_add_u32 s43, s43, 32768\n"
    "Lwk_110_%=:\n"
    "s_add_u32 s44, s44, 1\n"
    "s_lshr_b32 s48, s48, 4\n"
    "s_cmp_lt_u32 s44, s42\n"
    "s_cbranch_scc1 Ltop0_%=\n"
    "Lo3_%=:\n"
    "v_add_u32 v40, s40, v3\n"
    "s_waitcnt vmcnt(0)\n"
    "ds_write_b128 v40, v[60:63] offset:0\n"
    "s_waitcnt lgkmcnt(14)\n"
    "ds_write_b128 v40, v[92:95] offset:1056\n"
    "s_waitcnt lgkmcnt(14)\n"
    "ds_write_b128 v40, v[124:127] offset:8448\n"
    "s_waitcnt lgkmcnt(14)\n"
    "ds_write_b128 v40, v[68:71] offset:9504\n"
    "s_waitcnt lgkmcnt(14)\n"
    "ds_write_b128 v40, v[100:103] offset:16896\n"
    "s_waitcnt lgkmcnt(14)\n"
    "ds_write_b128 v40, v[132:135] offset:17952\n"
    "s_waitcnt lgkmcnt(14)\n"
    "ds_write_b128 v40, v[76:79] offset:25344\n"
    "s_waitcnt lgkmcnt(14)\n"
    "ds_write_b128 v40, v[108:111] offset:26400\n"
    "s_waitcnt lgkmcnt(14)\n"
    "ds_write_b128 v40, v[140:143] offset:33792\n"
    "s_waitcnt lgkmcnt(14)\n"
    "ds_write_b128 v40, v[52:55] offset:34848\n"
    "s_waitcnt lgkmcnt(14)\n"
    "ds_write_b128 v40, v[84:87] offset:42240\n"
    "s_waitcnt lgkmcnt(14)\n"
    "ds_write_b128 v40, v[116:119] offset:43296\n"
    "s_mul_i32 s54, %[team], -152064\n"
    "s_add_u32 s54, s54, 76032\n"
    "v_add_u32 v41, s54, v40\n"
    "s_waitcnt lgkmcnt(0)\n"
    "ds_write_b32 v20, v6\n"
    "s_mov_b64 s[68:69], %[ws]\n"
    "s_add_u32 s68, s68, 26214400\n"
    "s_addc_u32 s69, s69, 0\n"
    "s_and_b32 s69, s69, 0xffff\n"
    "s_mov_b32 s70, 18874368\n"
    "s_mov_b32 s71, 0x20000\n"
    "s_add_u32 s55, %[inbox], 1024\n"
    "s_and_b32 s55, s55, 0xc00\n"
    "s_andn2_b32 s56, %[inbox], 0xc00\n"
    "s_or_b32 s55, s55, s56\n"
    "s_mul_i32 s64, s55, 72\n"
    "s_add_u32 s64, s64, 0\n"
    "s_add_u32 s55, %[inbox], 2048\n"
    "s_and_b32 s55, s55, 0xc00\n"
    "s_andn2_b32 s56, %[inbox], 0xc00\n"
    "s_or_b32 s55, s55, s56\n"
    "s_mul_i32 s65, s55, 72\n"
    "s_add_u32 s65, s65, 3072\n"
    "s_add_u32 s55, %[inbox], 3072\n"
    "s_and_b32 s55, s55, 0xc00\n"
    "s_andn2_b32 s56, %[inbox], 0xc00\n"
    "s_or_b32 s55, s55, s56\n"
    "s_mul_i32 s66, s55, 72\n"
    "s_add_u32 s66, s66, 6144\n"
    "s_mul_i32 s41, %[inbox], 72\n"
    "s_mov_b64 s[36:37], %[rp]\n"
    "s_and_b32 s37, s37, 0xffff\n"
    "s_mov_b32 s38, 96288\n"
    "s_mov_b32 s39, 0x20000\n"
    "v_lshrrev_b32 v42, 4, v3\n"
    "v_and_b32 v45, 15, v42\n"
    "v_lshlrev_b32 v40, 11, v45\n"
    "v_lshrrev_b32 v45, 4, v42\n"
    "v_lshl_or_b32 v40, v45, 3, v40\n"
    "Lx1_%=:\n"
    "ds_read_b32 v22, v21\n"
    "s_add_u32 s51, s51, 16\n"
    "s_cmp_gt_u32 s51, 0x400000\n"
    "s_cbranch_scc1 Lx2_%=\n"
    "s_waitcnt lgkmcnt(0)\n"
    "v_cmp_ne_u32 vcc, 0, v22\n"
    "s_cbranch_vccnz Lx2_%=\n"
    "s_sleep 1\n"
    "s_and_b32 s54, s51, 0x3f0\n"
    "s_cbranch_scc1 Lx1_%=\n"
    "global_load_dword v16, v12, %[ws] sc1\n"
    "s_waitcnt vmcnt(0)\n"
    "s_call_b64 s[52:53], Lhs_%=\n"
    "s_branch Lx1_%=\n"
    "Lx2_%=:\n"
    "s_waitcnt lgkmcnt(0)\n"
    "ds_read_b128 v[60:63], v41 offset:0\n"
    "ds_read_b128 v[92:95], v41 offset:1056\n"
    "ds_read_b128 v[124:127], v41 offset:8448\n"
    "ds_read_b128 v[68:71], v41 offset:9504\n"
    "ds_read_b128 v[100:103], v41 offset:16896\n"
    "ds_read_b128 v[132:135], v41 offset:17952\n"
    "ds_read_b128 v[76:79], v41 offset:25344\n"
    "ds_read_b128 v[108:111], v41 offset:26400\n"
    "ds_read_b128 v[140:143], v41 offset:33792\n"
    "ds_read_b128 v[52:55], v41 offset:34848\n"
    "ds_read_b128 v[84:87], v41 offset:42240\n"
    "ds_read_b128 v[116:119], v41 offset:43296\n"
    "v_lshrrev_b32 v44, 1, v3\n"
    "s_waitcnt lgkmcnt(11)\n"
    "v_pk_add_f32 v[56:57], v[56:57], v[60:61]\n"
    "v_pk_add_f32 v[58:59], v[58:59], v[62:63]\n"
    "v_cvt_pk_bf16_f32 v60, v56, v57\n"
    "v_cvt_pk_bf16_f32 v61, v58, v59\n"
    "s_waitcnt lgkmcnt(10)\n"
    "v_pk_add_f32 v[88:89], v[88:89], v[92:93]\n"
    "v_pk_add_f32 v[90:91], v[90:91], v[94:95]\n"
    "v_cvt_pk_bf16_f32 v62, v88, v89\n"
    "v_cvt_pk_bf16_f32 v63, v90, v91\n"
    "buffer_store_dwordx4 v[60:63], v3, s[68:71], s64 offen sc1\n"
    "s_waitcnt lgkmcnt(9)\n"
    "v_pk_add_f32 v[120:121], v[120:121], v[124:125]\n"
    "v_pk_add_f32 v[122:123], v[122:123], v[126:127]\n"
    "v_cvt_pk_bf16_f32 v124, v120, v121\n"
    "v_cvt_pk_bf16_f32 v125, v122, v123\n"
    "buffer_store_dwordx2 v[124:125], v44, s[68:71], s64 offen offset:1024 sc1\n"
    "s_waitcnt lgkmcnt(8)\n"
    "v_pk_add_f32 v[64:65], v[64:65], v[68:69]\n"
    "v_pk_add_f32 v[66:67], v[66:67], v[70:71]\n"
    "v_cvt_pk_bf16_f32 v68, v64, v65\n"
    "v_cvt_pk_bf16_f32 v69, v66, v67\n"
    "s_waitcnt lgkmcnt(7)\n"
    "v_pk_add_f32 v[96:97], v[96:97], v[100:101]\n"
    "v_pk_add_f32 v[98:99], v[98:99], v[102:103]\n"
    "v_cvt_pk_bf16_f32 v70, v96, v97\n"
    "v_cvt_pk_bf16_f32 v71, v98, v99\n"
    "buffer_store_dwordx4 v[68:71], v3, s[68:71], s65 offen sc1\n"
    "s_waitcnt lgkmcnt(6)\n"
    "v_pk_add_f32 v[128:129], v[128:129], v[132:133]\n"
    "v_pk_add_f32 v[130:131], v[130:131], v[134:135]\n"
    "v_cvt_pk_bf16_f32 v132, v128, v129\n"
    "v_cvt_pk_bf16_f32 v133, v130, v131\n"
    "buffer_store_dwordx2 v[132:133], v44, s[68:71], s65 offen offset:1024 sc1\n"
    "s_waitcnt lgkmcnt(5)\n"
    "v_pk_add_f32 v[72:73], v[72:73], v[76:77]\n"
    "v_pk_add_f32 v[74:75], v[74:75], v[78:79]\n"
    "v_cvt_pk_bf16_f32 v76, v72, v73\n"
    "v_cvt_pk_bf16_f32 v77, v74, v75\n"
    "s_waitcnt lgkmcnt(4)\n"
    "v_pk_add_f32 v[104:105], v[104:105], v[108:109]\n"
    "v_pk_add_f32 v[106:107], v[106:107], v[110:111]\n"
    "v_cvt_pk_bf16_f32 v78, v104, v105\n"
    "v_cvt_pk_bf16_f32 v79, v106, v107\n"
    "buffer_store_dwordx4 v[76:79], v3, s[68:71], s66 offen sc1\n"
    "s_waitcnt lgkmcnt(3)\n"
    "v_pk_add_f32 v[136:137], v[136:137], v[140:141]\n"
    "v_pk_add_f32 v[138:139], v[138:139], v[142:143]\n"
    "v_cvt_pk_bf16_f32 v140, v136, v137\n"
    "v_cvt_pk_bf16_f32 v141, v138, v139\n"
    "buffer_store_dwordx2 v[140:141], v44, s[68:71], s66 offen offset:1024 sc1\n"
    "s_waitcnt lgkmcnt(2)\n"
    "v_pk_add_f32 v[48:49], v[48:49], v[52:53]\n"
    "v_pk_add_f32 v[50:51], v[50:51], v[54:55]\n"
    "s_waitcnt lgkmcnt(1)\n"
    "v_pk_add_f32 v[80:81], v[80:81], v[84:85]\n"
    "v_pk_add_f32 v[82:83], v[82:83], v[86:87]\n"
    "s_waitcnt lgkmcnt(0)\n"
    "v_pk_add_f32 v[112:113], v[112:113], v[116:117]\n"
    "v_pk_add_f32 v[114:115], v[114:115], v[118:119]\n"
    "s_waitcnt vmcnt(4)\n"
    "s_mov_b64 exec, 0x1000\n"
    "v_bfi_b32 v18, v15, v14, v16\n"
    "global_store_dword v13, v18, %[ws] sc1\n"
    "s_mov_b64 exec, -1\n"
    "s_waitcnt vmcnt(3)\n"
    "s_mov_b64 exec, 0x2000\n"
    "v_bfi_b32 v18, v15, v14, v16\n"
    "global_store_dword v13, v18, %[ws] sc1\n"
    "s_mov_b64 exec, -1\n"
    "s_waitcnt vmcnt(2)\n"
    "s_mov_b64 exec, 0x4000\n"
    "v_bfi_b32 v18, v15, v14, v16\n"
    "global_store_dword v13, v18, %[ws] sc1\n"
    "s_mov_b64 exec, -1\n"
    "s_or_b32 s84, s84, 0x7000\n"
    "s_mov_b32 s45, 0x70\n"
    "s_branch Lr3_%=\n"
    "Lr0_%=:\n"
    "global_load_dword v16, v12, %[ws] sc1\n"
    "s_add_u32 s51, s51, 64\n"
    "s_cmp_gt_u32 s51, 0x400000\n"
    "s_cbranch_scc1 Lr1_%=\n"
    "s_waitcnt vmcnt(0)\n"
    "s_call_b64 s[52:53], Lhs_%=\n"
    "Lr3_%=:\n"
    "s_andn2_b32 s78, s45, s84\n"
    "s_bitcmp0_b32 s78, 4\n"
    "s_cbranch_scc1 Lrs0_%=\n"
    "s_add_u32 s54, s41, 0\n"
    "buffer_load_dwordx4 v[144:147], v3, s[68:71], s54 offen sc1\n"
    "buffer_load_dwordx2 v[148:149], v44, s[68:71], s54 offen offset:1024 sc1\n"
    "Lrs0_%=:\n"
    "s_bitcmp0_b32 s78, 5\n"
    "s_cbranch_scc1 Lrs1_%=\n"
    "s_add_u32 s54, s41, 3072\n"
    "buffer_load_dwordx4 v[156:159], v3, s[68:71], s54 offen sc1\n"
    "buffer_load_dwordx2 v[160:161], v44, s[68:71], s54 offen offset:1024 sc1\n"
    "Lrs1_%=:\n"
    "s_bitcmp0_b32 s78, 6\n"
    "s_cbranch_scc1 Lrs2_%=\n"
    "s_add_u32 s54, s41, 6144\n"
    "buffer_load_dwordx4 v[168:171], v3, s[68:71], s54 offen sc1\n"
    "buffer_load_dwordx2 v[172:173], v44, s[68:71], s54 offen offset:1024 sc1\n"
    "Lrs2_%=:\n"
    "s_and_b32 s45, s45, s84\n"
    "s_cbranch_scc1 Lr0_%=\n"
    "Lr1_%=:\n"
    "s_waitcnt vmcnt(0)\n"
    "v_lshlrev_b32 v56, 16, v144\n"
    "v_and_b32 v57, 0xffff0000, v144\n"
    "v_lshlrev_b32 v58, 16, v145\n"
    "v_and_b32 v59, 0xffff0000, v145\n"
    "v_lshlrev_b32 v60, 16, v156\n"
    "v_and_b32 v61, 0xffff0000, v156\n"
    "v_lshlrev_b32 v62, 16, v157\n"
    "v_and_b32 v63, 0xffff0000, v157\n"
    "v_lshlrev_b32 v64, 16, v168\n"
    "v_and_b32 v65, 0xffff0000, v168\n"
    "v_lshlrev_b32 v66, 16, v169\n"
    "v_and_b32 v67, 0xffff0000, v169\n"
    "v_pk_add_f32 v[48:49], v[48:49], v[56:57]\n"
    "v_pk_add_f32 v[50:51], v[50:51], v[58:59]\n"
    "v_pk_add_f32 v[60:61], v[60:61], v[64:65]\n"
    "v_pk_add_f32 v[62:63], v[62:63], v[66:67]\n"
    "v_pk_add_f32 v[48:49], v[48:49], v[60:61]\n"
    "v_pk_add_f32 v[50:51], v[50:51], v[62:63]\n"
    "v_cvt_pk_bf16_f32 v52, v48, v49\n"
    "v_cvt_pk_bf16_f32 v53, v50, v51\n"
    "s_mov_b32 s54, 0\n"
    "buffer_store_dwordx2 v[52:53], v40, s[36:39], s54 offen\n"
    "v_lshlrev_b32 v88, 16, v146\n"
    "v_and_b32 v89, 0xffff0000, v146\n"
    "v_lshlrev_b32 v90, 16, v147\n"
    "v_and_b32 v91, 0xffff0000, v147\n"
    "v_lshlrev_b32 v92, 16, v158\n"
    "v_and_b32 v93, 0xffff0000, v158\n"
    "v_lshlrev_b32 v94, 16, v159\n"
    "v_and_b32 v95, 0xffff0000, v159\n"
    "v_lshlrev_b32 v96, 16, v170\n"
    "v_and_b32 v97, 0xffff0000, v170\n"
    "v_lshlrev_b32 v98, 16, v171\n"
    "v_and_b32 v99, 0xffff0000, v171\n"
    "v_pk_add_f32 v[80:81], v[80:81], v[88:89]\n"
    "v_pk_add_f32 v[82:83], v[82:83], v[90:91]\n"
    "v_pk_add_f32 v[92:93], v[92:93], v[96:97]\n"
    "v_pk_add_f32 v[94:95], v[94:95], v[98:99]\n"
    "v_pk_add_f32 v[80:81], v[80:81], v[92:93]\n"
    "v_pk_add_f32 v[82:83], v[82:83], v[94:95]\n"
    "v_cvt_pk_bf16_f32 v84, v80, v81\n"
    "v_cvt_pk_bf16_f32 v85, v82, v83\n"
    "s_mov_b32 s54, 32768\n"
    "buffer_store_dwordx2 v[84:85], v40, s[36:39], s54 offen\n"
    "v_lshlrev_b32 v120, 16, v148\n"
    "v_and_b32 v121, 0xffff0000, v148\n"
    "v_lshlrev_b32 v122, 16, v149\n"
    "v_and_b32 v123, 0xffff0000, v149\n"
    "v_lshlrev_b32 v124, 16, v160\n"
    "v_and_b32 v125, 0xffff0000, v160\n"
    "v_lshlrev_b32 v126, 16, v161\n"
    "v_and_b32 v127, 0xffff0000, v161\n"
    "v_lshlrev_b32 v128, 16, v172\n"
    "v_and_b32 v129, 0xffff0000, v172\n"
    "v_lshlrev_b32 v130, 16, v173\n"
    "v_and_b32 v131, 0xffff0000, v173\n"
    "v_pk_add_f32 v[112:113], v[112:113], v[120:121]\n"
    "v_pk_add_f32 v[114:115], v[114:115], v[122:123]\n"
    "v_pk_add_f32 v[124:125], v[124:125], v[128:129]\n"
    "v_pk_add_f32 v[126:127], v[126:127], v[130:131]\n"
    "v_pk_add_f32 v[112:113], v[112:113], v[124:125]\n"
    "v_pk_add_f32 v[114:115], v[114:115], v[126:127]\n"
    "v_cvt_pk_bf16_f32 v116, v112, v113\n"
    "v_cvt_pk_bf16_f32 v117, v114, v115\n"
    "s_mov_b32 s54, 65536\n"
    "buffer_store_dwordx2 v[116:117], v40, s[36:39], s54 offen\n"
    "Lfin0_%=:\n"
    "s_and_b32 s84, s84, 0xff00\n"
    "s_cbranch_scc0 Lfin0x_%=\n"
    "global_load_dword v16, v12, %[ws] sc1\n"
    "s_add_u32 s51, s51, 64\n"
    "s_cmp_gt_u32 s51, 0x400000\n"
    "s_cbranch_scc1 Lfin0x_%=\n"
    "s_waitcnt vmcnt(0)\n"
    "s_call_b64 s[52:53], Lhs_%=\n"
    "s_branch Lfin0_%=\n"
    "Lfin0x_%=:\n"
    "s_waitcnt vmcnt(0) lgkmcnt(0)\n"
    "s_branch Lend_%=\n"
    "Lst0_%=:\n"
    "s_bfe_u32 s41, s48, 196608\n"
    "s_mul_i32 s60, s41, 49152\n"
    "s_add_u32 s61, s60, 4096\n"
    "s_add_u32 s62, s60, 8192\n"
    "s_or_b32 s54, s84, s78\n"
    "s_lshl_b32 s54, s54, 1\n"
    "s_bitcmp0_b32 s54, s44\n"
    "s_cbranch_scc1 Lsb0_%=\n"
    "Lsq0_%=:\n"
    "global_load_dword v16, v12, %[ws] sc1\n"
    "s_add_u32 s51, s51, 64\n"
    "s_cmp_gt_u32 s51, 0x400000\n"
    "s_cbranch_scc1 Lsb0_%=\n"
    "s_waitcnt vmcnt(0) lgkmcnt(0)\n"
    "s_call_b64 s[52:53], Lhs_%=\n"
    "s_or_b32 s54, s84, s78\n"
    "s_lshl_b32 s54, s54, 1\n"
    "s_bitcmp0_b32 s54, s44\n"
    "s_cbranch_scc1 Lsb0_%=\n"
    "s_sleep 2\n"
    "s_branch Lsq0_%=\n"
    "Lsb0_%=:\n"
    "buffer_load_dwordx4 v[144:147], v3, s[36:39], s60 offen offset:0 sc1\n"
    "buffer_load_dwordx4 v[148:151], v3, s[36:39], s61 offen offset:0 sc1\n"
    "buffer_load_dwordx4 v[152:155], v3, s[36:39], s62 offen offset:0 sc1\n"
    "buffer_load_dwordx4 v[156:159], v3, s[36:39], s60 offen offset:1024 sc1\n"
    "buffer_load_dwordx4 v[160:163], v3, s[36:39], s61 offen offset:1024 sc1\n"
    "buffer_load_dwordx4 v[164:167], v3, s[36:39], s62 offen offset:1024 sc1\n"
    "buffer_load_dwordx4 v[168:171], v3, s[36:39], s60 offen offset:2048 sc1\n"
    "buffer_load_dwordx4 v[172:175], v3, s[36:39], s61 offen offset:2048 sc1\n"
    "buffer_load_dwordx4 v[176:179], v3, s[36:39], s62 offen offset:2048 sc1\n"
    "buffer_load_dwordx4 v[180:183], v3, s[36:39], s60 offen offset:3072 sc1\n"
    "buffer_load_dwordx4 v[184:187], v3, s[36:39], s61 offen offset:3072 sc1\n"
    "buffer_load_dwordx4 v[188:191], v3, s[36:39], s62 offen offset:3072 sc1\n"
    "s_bfe_u32 s41, s48, 196612\n"
    "s_mul_i32 s60, s41, 49152\n"
    "s_add_u32 s61, s60, 4096\n"
    "s_add_u32 s62, s60, 8192\n"
    "s_or_b32 s54, s84, s78\n"
    "s_bitcmp0_b32 s54, s44\n"
    "s_cbranch_scc1 Lsd0_%=\n"
    "s_waitcnt vmcnt(0)\n"
    "s_branch Lgo0_%=\n"
    "Lsd0_%=:\n"
    "buffer_load_dwordx4 a[32:35], v3, s[36:39], s60 offen offset:0 sc1\n"
    "buffer_load_dwordx4 a[36:39], v3, s[36:39], s61 offen offset:0 sc1\n"
    "buffer_load_dwordx4 a[40:43], v3, s[36:39], s62 offen offset:0 sc1\n"
    "buffer_load_dwordx4 a[44:47], v3, s[36:39], s60 offen offset:1024 sc1\n"
    "buffer_load_dwordx4 a[48:51], v3, s[36:39], s61 offen offset:1024 sc1\n"
    "buffer_load_dwordx4 a[52:55], v3, s[36:39], s62 offen offset:1024 sc1\n"
    "buffer_load_dwordx4 a[56:59], v3, s[36:39], s60 offen offset:2048 sc1\n"
    "buffer_load_dwordx4 a[60:63], v3, s[36:39], s61 offen offset:2048 sc1\n"
    "buffer_load_dwordx4 v[24:27], v3, s[36:39], s62 offen offset:2048 sc1\n"
    "buffer_load_dwordx4 v[28:31], v3, s[36:39], s60 offen offset:3072 sc1\n"
    "buffer_load_dwordx4 v[32:35], v3, s[36:39], s61 offen offset:3072 sc1\n"
    "buffer_load_dwordx4 v[36:39], v3, s[36:39], s62 offen offset:3072 sc1\n"
    "s_mov_b32 s76, 0\n"
    "s_mov_b32 s45, 1\n"
    "s_waitcnt vmcnt(12)\n"
    "s_branch Lgp0_%=\n"
    "Lst1_%=:\n"
    "s_bfe_u32 s41, s48, 196608\n"
    "s_mul_i32 s60, s41, 49152\n"
    "s_add_u32 s61, s60, 4096\n"
    "s_add_u32 s62, s60, 8192\n"
    "s_or_b32 s54, s84, s78\n"
    "s_lshl_b32 s54, s54, 1\n"
    "s_bitcmp0_b32 s54, s44\n"
    "s_cbranch_scc1 Lsb1_%=\n"
    "Lsq1_%=:\n"
    "global_load_dword v16, v12, %[ws] sc1\n"
    "s_add_u32 s51, s51, 64\n"
    "s_cmp_gt_u32 s51, 0x400000\n"
    "s_cbranch_scc1 Lsb1_%=\n"
    "s_waitcnt vmcnt(0) lgkmcnt(0)\n"
    "s_call_b64 s[52:53], Lhs_%=\n"
    "s_or_b32 s54, s84, s78\n"
    "s_lshl_b32 s54, s54, 1\n"
    "s_bitcmp0_b32 s54, s44\n"
    "s_cbranch_scc1 Lsb1_%=\n"
    "s_sleep 2\n"
    "s_branch Lsq1_%=\n"
    "Lsb1_%=:\n"
    "buffer_load_dwordx4 a[32:35], v3, s[36:39], s60 offen offset:0 sc1\n"
    "buffer_load_dwordx4 a[36:39], v3, s[36:39], s61 offen offset:0 sc1\n"
    "buffer_load_dwordx4 a[40:43], v3, s[36:39], s62 offen offset:0 sc1\n"
    "buffer_load_dwordx4 a[44:47], v3, s[36:39], s60 offen offset:1024 sc1\n"
    "buffer_load_dwordx4 a[48:51], v3, s[36:39], s61 offen offset:1024 sc1\n"
    "buffer_load_dwordx4 a[52:55], v3, s[36:39], s62 offen offset:1024 sc1\n"
    "buffer_load_dwordx4 a[56:59], v3, s[36:39], s60 offen offset:2048 sc1\n"
    "buffer_load_dwordx4 a[60:63], v3, s[36:39], s61 offen offset:2048 sc1\n"
    "buffer_load_dwordx4 v[24:27], v3, s[36:39], s62 offen offset:2048 sc1\n"
    "buffer_load_dwordx4 v[28:31], v3, s[36:39], s60 offen offset:3072 sc1\n"
    "buffer_load_dwordx4 v[32:35], v3, s[36:39], s61 offen offset:3072 sc1\n"
    "buffer_load_dwordx4 v[36:39], v3, s[36:39], s62 offen offset:3072 sc1\n"
    "s_bfe_u32 s41, s48, 196612\n"
    "s_mul_i32 s60, s41, 49152\n"
    "s_add_u32 s61, s60, 4096\n"
    "s_add_u32 s62, s60, 8192\n"
    "s_or_b32 s54, s84, s78\n"
    "s_bitcmp0_b32 s54, s44\n"
    "s_cbranch_scc1 Lsd1_%=\n"
    "s_waitcnt vmcnt(0)\n"
    "s_branch Lgo1_%=\n"
    "Lsd1_%=:\n"
    "buffer_load_dwordx4 v[144:147], v3, s[36:39], s60 offen offset:0 sc1\n"
    "buffer_load_dwordx4 v[148:151], v3, s[36:39], s61 offen offset:0 sc1\n"
    "buffer_load_dwordx4 v[152:155], v3, s[36:39], s62 offen offset:0 sc1\n"
    "buffer_load_dwordx4 v[156:159], v3, s[36:39], s60 offen offset:1024 sc1\n"
    "buffer_load_dwordx4 v[160:163], v3, s[36:39], s61 offen offset:1024 sc1\n"
    "buffer_load_dwordx4 v[164:167], v3, s[36:39], s62 offen offset:1024 sc1\n"
    "buffer_load_dwordx4 v[168:171], v3, s[36:39], s60 offen offset:2048 sc1\n"
    "buffer_load_dwordx4 v[172:175], v3, s[36:39], s61 offen offset:2048 sc1\n"
    "buffer_load_dwordx4 v[176:179], v3, s[36:39], s62 offen offset:2048 sc1\n"
    "buffer_load_dwordx4 v[180:183], v3, s[36:39], s60 offen offset:3072 sc1\n"
    "buffer_load_dwordx4 v[184:187], v3, s[36:39], s61 offen offset:3072 sc1\n"
    "buffer_load_dwordx4 v[188:191], v3, s[36:39], s62 offen offset:3072 sc1\n"
    "s_mov_b32 s76, 0\n"
    "s_mov_b32 s45, 1\n"
    "s_waitcnt vmcnt(12)\n"
    "s_branch Lgp1_%=\n"
    "Lslow_%=:\n"
    "s_cmp_lg_u32 s49, 0\n"
    "s_cbranch_scc1 Ldone_%=\n"
    "s_mov_b32 s49, 1\n"
    "s_waitcnt vmcnt(0) lgkmcnt(0)\n"
    "v_lshrrev_b32 v11, 8, v3\n"
    "v_and_b32 v3, 0xf0, v3\n"
    "v_lshl_or_b32 v3, v11, 15, v3\n"
    "v_xor_b32 v11, %[w], v11\n"
    "v_lshl_or_b32 v3, v11, 8, v3\n"
    "ds_add_u32 v4, v6\n"
    "s_add_u32 s46, s46, 1\n"
    "s_call_b64 s[52:53], Lspin_%=\n"
    "v_mov_b32 v8, 0xff800000\n"
    "v_mov_b32 v9, 0xff800000\n"
    "v_mov_b32 v10, 0xff800000\n"
    "s_mov_b32 s41, s59\n"
    "s_mov_b32 s45, 32\n"
    "Lsl_%=:\n"
    "s_add_u32 m0, s40, 0\n"
    "s_xor_b32 s77, s41, 0x2000\n"
    "buffer_load_dwordx4 v3, s[20:23], s41 offen nt lds\n"
    "s_add_u32 m0, m0, 1056\n"
    "s_add_u32 s41, s41, 107520\n"
    "buffer_load_dwordx4 v3, s[20:23], s77 offen nt lds\n"
    "s_and_b32 s41, s41, 0x25cff\n"
    "s_waitcnt vmcnt(0)\n"
    "ds_add_u32 v4, v6\n"
    "s_add_u32 s46, s46, 1\n"
    "s_call_b64 s[52:53], Lspin_%=\n"
    "ds_read_b128 a[0:3], v1 offset:0\n"
    "ds_read_b128 a[4:7], v1 offset:64\n"
    "ds_read_b128 a[8:11], v1 offset:128\n"
    "ds_read_b128 a[12:15], v1 offset:192\n"
    "ds_read_b128 a[28:31], v1 offset:704\n"
    "ds_read_b128 a[24:27], v1 offset:640\n"
    "ds_read_b128 a[20:23], v1 offset:576\n"
    "ds_read_b128 a[16:19], v1 offset:512\n"
    "s_waitcnt lgkmcnt(0)\n"
    "ds_add_u32 v4, v6\n"
    "s_add_u32 s46, s46, 1\n"
    "s_call_b64 s[52:53], Lspin_%=\n"
    "v_mfma_f32_16x16x32_bf16 v[24:27], a[0:3], v[144:147], 0\n"
    "v_mfma_f32_16x16x32_bf16 v[24:27], a[4:7], v[148:151], v[24:27]\n"
    "v_mfma_f32_16x16x32_bf16 v[24:27], a[8:11], v[152:155], v[24:27]\n"
    "v_mfma_f32_16x16x32_bf16 v[24:27], a[12:15], v[156:159], v[24:27]\n"
    "v_mfma_f32_16x16x32_bf16 v[28:31], a[28:31], v[156:159], 0\n"
    "v_mfma_f32_16x16x32_bf16 v[28:31], a[24:27], v[152:155], v[28:31]\n"
    "v_mfma_f32_16x16x32_bf16 v[28:31], a[20:23], v[148:151], v[28:31]\n"
    "v_mfma_f32_16x16x32_bf16 v[28:31], a[16:19], v[144:147], v[28:31]\n"
    "v_mfma_f32_16x16x32_bf16 v[32:35], a[0:3], v[160:163], 0\n"
    "v_mfma_f32_16x16x32_bf16 v[32:35], a[4:7], v[164:167], v[32:35]\n"
    "v_mfma_f32_16x16x32_bf16 v[32:35], a[8:11], v[168:171], v[32:35]\n"
    "v_mfma_f32_16x16x32_bf16 v[32:35], a[12:15], v[172:175], v[32:35]\n"
    "v_mfma_f32_16x16x32_bf16 v[36:39], a[28:31], v[172:175], 0\n"
    "v_mfma_f32_16x16x32_bf16 v[36:39], a[24:27], v[168:171], v[36:39]\n"
    "v_mfma_f32_16x16x32_bf16 v[36:39], a[20:23], v[164:167], v[36:39]\n"
    "v_mfma_f32_16x16x32_bf16 v[36:39], a[16:19], v[160:163], v[36:39]\n"
    "v_mfma_f32_16x16x32_bf16 v[40:43], a[0:3], v[176:179], 0\n"
    "v_mfma_f32_16x16x32_bf16 v[40:43], a[4:7], v[180:183], v[40:43]\n"
    "v_mfma_f32_16x16x32_bf16 v[40:43], a[8:11], v[184:187], v[40:43]\n"
    "v_mfma_f32_16x16x32_bf16 v[40:43], a[12:15], v[188:191], v[40:43]\n"
    "v_mfma_f32_16x16x32_bf16 v[44:47], a[28:31], v[188:191], 0\n"
    "v_mfma_f32_16x16x32_bf16 v[44:47], a[24:27], v[184:187], v[44:47]\n"
    "v_mfma_f32_16x16x32_bf16 v[44:47], a[20:23], v[180:183], v[44:47]\n"
    "v_mfma_f32_16x16x32_bf16 v[44:47], a[16:19], v[176:179], v[44:47]\n"
    "s_nop 7\n"
    "v_max3_f32 v8, v8, v24, v25\n"
    "v_max3_f32 v8, v8, v26, v27\n"
    "v_max3_f32 v8, v8, v28, v29\n"
    "v_max3_f32 v8, v8, v30, v31\n"
    "v_max3_f32 v9, v9, v32, v33\n"
    "v_max3_f32 v9, v9, v34, v35\n"
    "v_max3_f32 v9, v9, v36, v37\n"
    "v_max3_f32 v9, v9, v38, v39\n"
    "v_max3_f32 v10, v10, v40, v41\n"
    "v_max3_f32 v10, v10, v42, v43\n"
    "v_max3_f32 v10, v10, v44, v45\n"
    "v_max3_f32 v10, v10, v46, v47\n"
    "s_sub_u32 s45, s45, 1\n"
    "s_cmp_lg_u32 s45, 0\n"
    "s_cbranch_scc1 Lsl_%=\n"
    "s_mov_b32 s41, s59\n"
    "s_mov_b32 s42, s59\n"
    "s_add_u32 m0, s40, 0\n"
    "s_xor_b32 s77, s41, 0x2000\n"
    "buffer_load_dwordx4 v3, s[20:23], s41 offen nt lds\n"
    "s_add_u32 m0, m0, 1056\n"
    "s_add_u32 s41, s41, 107520\n"
    "buffer_load_dwordx4 v3, s[20:23], s77 offen nt lds\n"
    "s_and_b32 s41, s41, 0x25cff\n"
    "s_add_u32 m0, s40, 8448\n"
    "s_xor_b32 s77, s41, 0x2000\n"
    "buffer_load_dwordx4 v3, s[20:23], s41 offen nt lds\n"
    "s_add_u32 m0, m0, 1056\n"
    "s_add_u32 s41, s41, 107520\n"
    "buffer_load_dwordx4 v3, s[20:23], s77 offen nt lds\n"
    "s_and_b32 s41, s41, 0x25cff\n"
    "s_add_u32 m0, s40, 16896\n"
    "s_xor_b32 s77, s41, 0x2000\n"
    "buffer_load_dwordx4 v3, s[20:23], s41 offen nt lds\n"
    "s_add_u32 m0, m0, 1056\n"
    "s_add_u32 s41, s41, 107520\n"
    "buffer_load_dwordx4 v3, s[20:23], s77 offen nt lds\n"
    "s_and_b32 s41, s41, 0x25cff\n"
    "s_add_u32 m0, s40, 25344\n"
    "s_xor_b32 s77, s41, 0x2000\n"
    "buffer_load_dwordx4 v3, s[20:23], s41 offen nt lds\n"
    "s_add_u32 m0, m0, 1056\n"
    "s_add_u32 s41, s41, 107520\n"
    "buffer_load_dwordx4 v3, s[20:23], s77 offen nt lds\n"
    "s_and_b32 s41, s41, 0x25cff\n"
    "s_add_u32 m0, s40, 42240\n"
    "s_xor_b32 s77, s42, 0x2000\n"
    "buffer_load_dwordx4 v3, s[24:27], s42 offen nt lds\n"
    "s_add_u32 m0, m0, 1056\n"
    "s_add_u32 s42, s42, 107520\n"
    "buffer_load_dwordx4 v3, s[24:27], s77 offen nt lds\n"
    "s_and_b32 s42, s42, 0x25cff\n"
    "s_add_u32 m0, s40, 50688\n"
    "s_xor_b32 s77, s42, 0x2000\n"
    "buffer_load_dwordx4 v3, s[24:27], s42 offen nt lds\n"
    "s_add_u32 m0, m0, 1056\n"
    "s_add_u32 s42, s42, 107520\n"
    "buffer_load_dwordx4 v3, s[24:27], s77 offen nt lds\n"
    "s_and_b32 s42, s42, 0x25cff\n"
    "s_add_u32 m0, s40, 59136\n"
    "s_xor_b32 s77, s42, 0x2000\n"
    "buffer_load_dwordx4 v3, s[24:27], s42 offen nt lds\n"
    "s_add_u32 m0, m0, 1056\n"
    "s_add_u32 s42, s42, 107520\n"
    "buffer_load_dwordx4 v3, s[24:27], s77 offen nt lds\n"
    "s_and_b32 s42, s42, 0x25cff\n"
    "s_waitcnt vmcnt(0)\n"
    "ds_add_u32 v4, v6\n"
    "s_add_u32 s46, s46, 1\n"
    "s_call_b64 s[52:53], Lspin_%=\n"
    "ds_read_b128 a[0:3], v1 offset:0\n"
    "ds_read_b128 a[4:7], v1 offset:64\n"
    "ds_read_b128 a[8:11], v1 offset:128\n"
    "ds_read_b128 a[12:15], v1 offset:192\n"
    "ds_read_b128 a[28:31], v1 offset:704\n"
    "ds_read_b128 a[24:27], v1 offset:640\n"
    "ds_read_b128 a[20:23], v1 offset:576\n"
    "ds_read_b128 a[16:19], v1 offset:512\n"
    "s_waitcnt lgkmcnt(0)\n"
    "s_branch Lnegm_%=\n"
    "Lfs_2_%=:\n"
    "s_call_b64 s[52:53], Lspin_%=\n"
    "s_branch Lfb_1_%=\n"
    "Lfs_4_%=:\n"
    "s_call_b64 s[52:53], Lspin_%=\n"
    "s_branch Lfb_3_%=\n"
    "Lfs_6_%=:\n"
    "s_call_b64 s[52:53], Lspin_%=\n"
    "s_branch Lfb_5_%=\n"
    "Lfs_8_%=:\n"
    "s_call_b64 s[52:53], Lspin_%=\n"
    "s_branch Lfb_7_%=\n"
    "Lfs_10_%=:\n"
    "s_call_b64 s[52:53], Lspin_%=\n"
    "s_branch Lfb_9_%=\n"
    "Lfs_12_%=:\n"
    "s_call_b64 s[52:53], Lspin_%=\n"
    "s_branch Lfb_11_%=\n"
    "Lfs_14_%=:\n"
    "s_call_b64 s[52:53], Lspin_%=\n"
    "s_branch Lfb_13_%=\n"
    "Lfs_16_%=:\n"
    "s_call_b64 s[52:53], Lspin_%=\n"
    "s_branch Lfb_15_%=\n"
    "Lfs_18_%=:\n"
    "s_call_b64 s[52:53], Lspin_%=\n"
    "s_branch Lfb_17_%=\n"
    "Lfs_20_%=:\n"
    "s_call_b64 s[52:53], Lspin_%=\n"
    "s_branch Lfb_19_%=\n"
    "Lfs_22_%=:\n"
    "s_call_b64 s[52:53], Lspin_%=\n"
    "s_branch Lfb_21_%=\n"
    "Lfs_24_%=:\n"
    "s_call_b64 s[52:53], Lspin_%=\n"
    "s_branch Lfb_23_%=\n"
    "Lfs_26_%=:\n"
    "s_call_b64 s[52:53], Lspin_%=\n"
    "s_branch Lfb_25_%=\n"
    "Lfs_28_%=:\n"
    "s_call_b64 s[52:53], Lspin_%=\n"
    "s_branch Lfb_27_%=\n"
    "Lfs_30_%=:\n"
    "s_call_b64 s[52:53], Lspin_%=\n"
    "s_branch Lfb_29_%=\n"
    "Lfs_32_%=:\n"
    "s_call_b64 s[52:53], Lspin_%=\n"
    "s_branch Lfb_31_%=\n"
    "Ltsl_34_%=:\n"
    "s_call_b64 s[52:53], Lpace_%=\n"
    "s_branch Ltb_33_%=\n"
    "Lfs_36_%=:\n"
    "s_call_b64 s[52:53], Lspin_%=\n"
    "s_branch Lfb_35_%=\n"
    "Ltsl_42_%=:\n"
    "s_call_b64 s[52:53], Lpace_%=\n"
    "s_branch Ltb_41_%=\n"
    "Lfs_44_%=:\n"
    "s_call_b64 s[52:53], Lspin_%=\n"
    "s_branch Lfb_43_%=\n"
    "Ltsl_46_%=:\n"
    "s_call_b64 s[52:53], Lpace_%=\n"
    "s_branch Ltb_45_%=\n"
    "Lfs_48_%=:\n"
    "s_call_b64 s[52:53], Lspin_%=\n"
    "s_branch Lfb_47_%=\n"
    "Ltsl_50_%=:\n"
    "s_call_b64 s[52:53], Lpace_%=\n"
    "s_branch Ltb_49_%=\n"
    "Lfs_52_%=:\n"
    "s_call_b64 s[52:53], Lspin_%=\n"
    "s_branch Lfb_51_%=\n"
    "Ltsl_54_%=:\n"
    "s_call_b64 s[52:53], Lpace_%=\n"
    "s_branch Ltb_53_%=\n"
    "Lfs_56_%=:\n"
    "s_call_b64 s[52:53], Lspin_%=\n"
    "s_branch Lfb_55_%=\n"
    "Ltsl_58_%=:\n"
    "s_call_b64 s[52:53], Lpace_%=\n"
    "s_branch Ltb_57_%=\n"
    "Lfs_60_%=:\n"
    "s_call_b64 s[52:53], Lspin_%=\n"
    "s_branch Lfb_59_%=\n"
    "Ltsl_62_%=:\n"
    "s_call_b64 s[52:53], Lpace_%=\n"
    "s_branch Ltb_61_%=\n"
    "Lfs_64_%=:\n"
    "s_call_b64 s[52:53], Lspin_%=\n"
    "s_branch Lfb_63_%=\n"
    "Ltsl_66_%=:\n"
    "s_call_b64 s[52:53], Lpace_%=\n"
    "s_branch Ltb_65_%=\n"
    "Lfs_68_%=:\n"
    "s_call_b64 s[52:53], Lspin_%=\n"
    "s_branch Lfb_67_%=\n"
    "Lfs_70_%=:\n"
    "s_call_b64 s[52:53], Lspin_%=\n"
    "s_branch Lfb_69_%=\n"
    "Lfs_78_%=:\n"
    "s_call_b64 s[52:53], Lspin_%=\n"
    "s_branch Lfb_77_%=\n"
    "Lfs_81_%=:\n"
    "s_call_b64 s[52:53], Lspin_%=\n"
    "s_branch Lfb_80_%=\n"
    "Lfs_84_%=:\n"
    "s_call_b64 s[52:53], Lspin_%=\n"
    "s_branch Lfb_83_%=\n"
    "Lfs_87_%=:\n"
    "s_call_b64 s[52:53], Lspin_%=\n"
    "s_branch Lfb_86_%=\n"
    "Lhs_91_%=:\n"
    "s_mov_b32 exec_lo, s54\n"
    "s_mov_b32 exec_hi, 0\n"
    "global_store_dword v13, v17, %[ws] sc1\n"
    "v_mov_b32 v18, v17\n"
    "s_mov_b64 exec, -1\n"
    "s_branch Lhb_90_%=\n"
    "Lfs_100_%=:\n"
    "s_call_b64 s[52:53], Lspin_%=\n"
    "s_branch Lfb_99_%=\n"
    "Lfs_103_%=:\n"
    "s_call_b64 s[52:53], Lspin_%=\n"
    "s_branch Lfb_102_%=\n"
    "Lfs_106_%=:\n"
    "s_call_b64 s[52:53], Lspin_%=\n"
    "s_branch Lfb_105_%=\n"
    "Lfs_109_%=:\n"
    "s_call_b64 s[52:53], Lspin_%=\n"
    "s_branch Lfb_108_%=\n"
    "Lhs_113_%=:\n"
    "s_mov_b32 exec_lo, s54\n"
    "s_mov_b32 exec_hi, 0\n"
    "global_store_dword v13, v17, %[ws] sc1\n"
    "v_mov_b32 v18, v17\n"
    "s_mov_b64 exec, -1\n"
    "s_branch Lhb_112_%=\n"
    "Lspin_%=:\n"
    "s_add_u32 s51, s51, 1\n"
    "s_cmp_gt_u32 s51, 0x400000\n"
    "s_cbranch_scc1 Lspin_out_%=\n"
    "ds_read_b32 v7, v5\n"
    "s_waitcnt lgkmcnt(0)\n"
    "v_cmp_gt_u32 vcc, s46, v7\n"
    "s_cbranch_vccnz Lspin_%=\n"
    "Lspin_out_%=:\n"
    "s_setpc_b64 s[52:53]\n"
    "Lpace_%=:\n"
    "s_min_i32 s81, s81, 4000\n"
    "s_add_u32 s82, s80, s81\n"
    "Lpace1_%=:\n"
    "s_sleep 1\n"
    "s_memrealtime s[80:81]\n"
    "s_waitcnt lgkmcnt(0)\n"
    "s_sub_u32 s81, s82, s80\n"
    "s_cmp_gt_i32 s81, 0\n"
    "s_cbranch_scc1 Lpace1_%=\n"
    "s_setpc_b64 s[52:53]\n"
    "Lhs_%=:\n"
    "v_bfi_b32 v17, v15, v14, v16\n"
    "v_cmp_eq_u32 vcc, v17, v16\n"
    "v_cmp_ne_u32 s[54:55], v17, v18\n"
    "s_xor_b32 s55, vcc_lo, 0xff00\n"
    "s_or_b32 s54, s54, 0xff\n"
    "s_and_b32 s54, s54, s55\n"
    "s_and_b32 s54, s54, s84\n"
    "s_cbranch_scc0 Lhs1_%=\n"
    "s_mov_b32 exec_lo, s54\n"
    "s_mov_b32 exec_hi, 0\n"
    "global_store_dword v13, v17, %[ws] sc1\n"
    "v_mov_b32 v18, v17\n"
    "s_mov_b64 exec, -1\n"
    "Lhs1_%=:\n"
    "s_andn2_b32 s84, s84, vcc_lo\n"
    "s_setpc_b64 s[52:53]\n"
    "Lend_%=:\n"
    :
    : [kp] "s"(kp),
      [vp] "s"(vp),
      [wp] "s"(wp),
      [xp] "s"(xp),
      [op] "s"(op),
      [wsz] "s"(wsz),
      [lds] "s"(ldsb),
      [w] "s"(w),
      [team] "s"(team),
      [op2] "s"(op2),
      [wp2] "s"(wp2),
      [rp] "s"(rp),
      [ws] "s"(ws),
      [inbox] "s"(wg * 1024u + wave * 128u),
      [htab] "s"(htab),
      [dta] "s"(dta),
      [nsw] "s"(nsw),
      [wsz2] "s"(1048576u - cq * 256u - w * 2048u),
      [kst] "s"(kst),
      [kan] "s"(kan)
    : "v0","v1","v2","v3","v4","v5","v6","v7","v8","v9","v10","v11","v12","v13","v14","v15","v16","v17","v18","v19","v20","v21","v22","v23","v24","v25","v26","v27","v28","v29","v30","v31","v32","v33","v34","v35","v36","v37","v38","v39","v40","v41","v42","v43","v44","v45","v46","v47","v48","v49","v50","v51","v52","v53","v54","v55","v56","v57","v58","v59","v60","v61","v62","v63","v64","v65","v66","v67","v68","v69","v70","v71","v72","v73","v74","v75","v76","v77","v78","v79","v80","v81","v82","v83","v84","v85","v86","v87","v88","v89","v90","v91","v92","v93","v94","v95","v96","v97","v98","v99","v100","v101","v102","v103","v104","v105","v106","v107","v108","v109","v110","v111","v112","v113","v114","v115","v116","v117","v118","v119","v120","v121","v122","v123","v124","v125","v126","v127","v128","v129","v130","v131","v132","v133","v134","v135","v136","v137","v138","v139","v140","v141","v142","v143","v144","v145","v146","v147","v148","v149","v150","v151","v152","v153","v154","v155","v156","v157","v158","v159","v160","v161","v162","v163","v164","v165","v166","v167","v168","v169","v170","v171","v172","v173","v174","v175","v176","v177","v178","v179","v180","v181","v182","v183","v184","v185","v186","v187","v188","v189","v190","v191","a0","a1","a2","a3","a4","a5","a6","a7","a8","a9","a10","a11","a12","a13","a14","a15","a16","a17","a18","a19","a20","a21","a22","a23","a24","a25","a26","a27","a28","a29","a30","a31","a32","a33","a34","a35","a36","a37","a38","a39","a40","a41","a42","a43","a44","a45","a46","a47","a48","a49","a50","a51","a52","a53","a54","a55","a56","a57","a58","a59","a60","a61","a62","a63","s20","s21","s22","s23","s24","s25","s26","s27","s28","s29","s30","s31","s36","s37","s38","s39","s40","s41","s42","s43","s44","s45","s46","s47","s48","s49","s50","s51","s52","s53","s54","s55","s56","s57","s58","s59","s60","s61","s62","s64","s65","s66","s67","s68","s69","s70","s71","s72","s73","s76","s77","s78","s79","s80","s81","s82","s83","s84","m0","vcc","scc","memory");
}

extern "C" void kernel_launch(void* const* d_in, const int* in_sizes, int n_in, void* d_out, int out_size, void* d_ws, size_t ws_size,
                              hipStream_t stream) {
  mha_kernel<<<256, 512, 0, stream>>>((const char*)d_in[0], (const char*)d_in[1], (const char*)d_in[2], (const char*)d_in[3], (const char*)d_in[4],
                                      (const char*)d_in[5], (const char*)d_in[6], (const char*)d_in[7], (const char*)d_in[8], (const char*)d_in[9],
                                      (char*)d_ws + WS_BASE(ws_size), (char*)d_out);
}
```
